```python
import torch

def _softmax(x, axis=-1):
    return torch.softmax(x, dim=axis)
def _gelu(x):
    return torch.nn.functional.gelu(x, approximate="tanh")
def _silu(x):
    return torch.nn.functional.silu(x)
def _sigmoid(x):
    return torch.sigmoid(x)
def _layernorm(x, axis=-1, eps=1e-5):
    m = x.mean(dim=axis, keepdim=True)
    v = x.var(dim=axis, keepdim=True, unbiased=False)
    return (x - m) / torch.sqrt(v + eps)
def _scatter_add(tgt, idx, src, axis=0):
    return tgt.clone().index_add_(axis, idx, src)
def _segment_sum(src, seg, n):
    out = torch.zeros((n,) + src.shape[1:], dtype=src.dtype)
    return out.index_add_(0, seg, src)
def _pad(x, pw):
    flat = tuple(v for pair in reversed(pw) for v in pair)
    return torch.nn.functional.pad(x, flat)

kernel_name = 'concat_heads__gqa_block_cached__gqa_block_cached__near_ridge'

def setup_inputs():

    _w = (lambda a: (lambda t: t.to(torch.bfloat16) if t.is_floating_point() else t)(torch.as_tensor(a)))
    def _A_setup():
        B, Sq, D = 32, 64, 576
        Hq, Hkv, Sk, Dh = 32, 16, 512, 128
        assert Hq % Hkv == 0, (Hq, Hkv)
        return {
            "x": _w((lambda *s: torch.randn(*s, dtype=torch.bfloat16) * 0.8)(B, Sq, D)),
            "k_cache": _w((lambda *s: torch.randn(*s, dtype=torch.bfloat16) * 0.8)(B, Hkv, Sk, Dh)),
            "v_cache": _w((lambda *s: torch.randn(*s, dtype=torch.bfloat16) * 0.05)(B, Hkv, Sk, Dh)),
            "wq": _w((lambda *s: torch.randn(*s, dtype=torch.bfloat16) * 0.8)(D, Hq * Dh) * (D ** -0.5)),
            "wo": _w((lambda *s: torch.randn(*s, dtype=torch.bfloat16) * 0.05)(Hq * Dh, D) * ((Hq * Dh) ** -0.5)),
        }
    _A_in = _A_setup()

    def _B_setup():
        B, Sq, D = 32, 64, 576
        Hq, Hkv, Sk, Dh = 32, 16, 512, 128
        assert Hq % Hkv == 0, (Hq, Hkv)
        return {
            "x": _w((lambda *s: torch.randn(*s, dtype=torch.bfloat16) * 0.8)(B, Sq, D)),
            "k_cache": _w((lambda *s: torch.randn(*s, dtype=torch.bfloat16) * 0.8)(B, Hkv, Sk, Dh)),
            "v_cache": _w((lambda *s: torch.randn(*s, dtype=torch.bfloat16) * 0.05)(B, Hkv, Sk, Dh)),
            "wq": _w((lambda *s: torch.randn(*s, dtype=torch.bfloat16) * 0.8)(D, Hq * Dh) * (D ** -0.5)),
            "wo": _w((lambda *s: torch.randn(*s, dtype=torch.bfloat16) * 0.05)(Hq * Dh, D) * ((Hq * Dh) ** -0.5)),
        }
    _B_in = _B_setup()

    return {
        "A_x": _A_in["x"],
        "A_k_cache": _A_in["k_cache"],
        "A_v_cache": _A_in["v_cache"],
        "A_wq": _A_in["wq"],
        "A_wo": _A_in["wo"],
        "B_x": _B_in["x"],
        "B_k_cache": _B_in["k_cache"],
        "B_v_cache": _B_in["v_cache"],
        "B_wq": _B_in["wq"],
        "B_wo": _B_in["wo"],
    }

def reference(A_x, A_k_cache, A_v_cache, A_wq, A_wo, B_x, B_k_cache, B_v_cache, B_wq, B_wo):
    def _A_ref(x, k_cache, v_cache, wq, wo):
        B, Sq, D = x.shape
        Hq, Dh = 32, 128
        Hkv = k_cache.shape[1]
        rep = Hq // Hkv
        q = (lambda x, *d: x.permute(*d))((x @ wq).reshape(B, Sq, Hq, Dh), 0, 2, 1, 3)
        k = (lambda x, n, axis: torch.repeat_interleave(x, n, dim=axis))(k_cache, rep, axis=1)
        v = (lambda x, n, axis: torch.repeat_interleave(x, n, dim=axis))(v_cache, rep, axis=1)
        scores = torch.einsum("bhqd,bhkd->bhqk", q, k) * (Dh ** -0.5)
        p = _softmax(scores, axis=-1)
        o = torch.einsum("bhqk,bhkd->bhqd", p, v)
        return (lambda x, *d: x.permute(*d))(o, 0, 2, 1, 3).reshape(B, Sq, Hq * Dh) @ wo

    def _B_ref(x, k_cache, v_cache, wq, wo):
        B, Sq, D = x.shape
        Hq, Dh = 32, 128
        Hkv = k_cache.shape[1]
        rep = Hq // Hkv
        q = (lambda x, *d: x.permute(*d))((x @ wq).reshape(B, Sq, Hq, Dh), 0, 2, 1, 3)
        k = (lambda x, n, axis: torch.repeat_interleave(x, n, dim=axis))(k_cache, rep, axis=1)
        v = (lambda x, n, axis: torch.repeat_interleave(x, n, dim=axis))(v_cache, rep, axis=1)
        scores = torch.einsum("bhqd,bhkd->bhqk", q, k) * (Dh ** -0.5)
        p = _softmax(scores, axis=-1)
        o = torch.einsum("bhqk,bhkd->bhqd", p, v)
        return (lambda x, *d: x.permute(*d))(o, 0, 2, 1, 3).reshape(B, Sq, Hq * Dh) @ wo

    a = _A_ref(A_x, A_k_cache, A_v_cache, A_wq, A_wo)
    b = _B_ref(B_x, B_k_cache, B_v_cache, B_wq, B_wo)
    return torch.concatenate([a, b], axis=-1)
```

```cpp
#include <hip/hip_runtime.h>
#include <hip/hip_bf16.h>
#include <stdint.h>

typedef short bf16x8 __attribute__((ext_vector_type(8)));
typedef short s16x4 __attribute__((ext_vector_type(4)));
typedef float f32x16 __attribute__((ext_vector_type(16)));
typedef float f32x4 __attribute__((ext_vector_type(4)));
typedef unsigned u32x4 __attribute__((ext_vector_type(4)));
typedef unsigned u32x2 __attribute__((ext_vector_type(2)));
typedef __attribute__((address_space(3))) char* lds_ptr;
typedef __attribute__((address_space(3))) const char* lds_cptr;

constexpr int LDS_X = 0, LDS_QO = 73728, LDS_KV = 106496, LDS_TOTAL = 163840;
constexpr float QSCALE = 0.08838834764831845f * 1.4426950408889634f;
constexpr float THR = 8.0f;

#define SBAR() __builtin_amdgcn_sched_barrier(0)
#define PIN(x) asm volatile("" : "+v"(x))
#define MFMA32(a, b, c) __builtin_amdgcn_mfma_f32_32x32x16_bf16(a, b, c, 0, 0, 0)
#define MFMA16(a, b, c) __builtin_amdgcn_mfma_f32_16x16x32_bf16(a, b, c, 0, 0, 0)
#define MFMA16_ACC(c, a, b) asm volatile("v_mfma_f32_16x16x32_bf16 %0, %1, %2, %0" : "+v"(c) : "v"(a), "v"(b))
#define WAIT_BAR(N) asm volatile("s_waitcnt vmcnt(" #N ") lgkmcnt(0)\n\ts_barrier" ::: "memory")
#define LGKM_BAR() asm volatile("s_waitcnt lgkmcnt(0)\n\ts_barrier" ::: "memory")
#define BAR() asm volatile("s_barrier" ::: "memory")
#define WAITVM(N) asm volatile("s_waitcnt vmcnt(" #N ")" ::: "memory")
#define MX3(a, b, c) __builtin_fmaxf(__builtin_fmaxf((a), (b)), (c))

__device__ __forceinline__ unsigned cvtpk(float lo, float hi) {
    typedef float f32x2_t __attribute__((ext_vector_type(2)));
    typedef __bf16 bf16x2_t __attribute__((ext_vector_type(2)));
    return __builtin_bit_cast(unsigned, __builtin_convertvector((f32x2_t){lo, hi}, bf16x2_t));
}
__device__ __forceinline__ void dma1(unsigned voff, const char* sbase, unsigned lds_base) {
    unsigned sv;
    asm volatile("s_mov_b32 %0, m0\n\ts_mov_b32 m0, %3\n\ts_nop 0\n\tglobal_load_lds_dwordx4 %1, %2\n\ts_mov_b32 m0, %0"
                 : "=&s"(sv) : "v"(voff), "s"(sbase), "s"(lds_base) : "memory");
}
__device__ __forceinline__ void dma2(unsigned voff, const char* s0, const char* s1m, unsigned lds_base) {
    unsigned sv;
    asm volatile("s_mov_b32 %0, m0\n\ts_mov_b32 m0, %4\n\ts_nop 0\n\tglobal_load_lds_dwordx4 %1, %2\n\tglobal_load_lds_dwordx4 %1, %3 offset:1024\n\ts_mov_b32 m0, %0"
                 : "=&s"(sv) : "v"(voff), "s"(s0), "s"(s1m), "s"(lds_base) : "memory");
}
__device__ __forceinline__ void dma2nt(unsigned voff, const char* s0, const char* s1m, unsigned lds_base) {
    unsigned sv;
    asm volatile("s_mov_b32 %0, m0\n\ts_mov_b32 m0, %4\n\ts_nop 0\n\tglobal_load_lds_dwordx4 %1, %2 nt\n\tglobal_load_lds_dwordx4 %1, %3 offset:1024 nt\n\ts_mov_b32 m0, %0"
                 : "=&s"(sv) : "v"(voff), "s"(s0), "s"(s1m), "s"(lds_base) : "memory");
}
__device__ __forceinline__ int lane_id() { int l; asm volatile("v_mbcnt_lo_u32_b32 %0, -1, 0\n\tv_mbcnt_hi_u32_b32 %0, -1, %0" : "=v"(l)); return l; }
__device__ __forceinline__ bf16x8 ldsr128(lds_cptr p) { return *(const __attribute__((address_space(3))) bf16x8*)p; }
__device__ __forceinline__ s16x4 vtr(lds_cptr p) {
    return __builtin_bit_cast(s16x4, __builtin_amdgcn_ds_read_tr16_b64_v4i16((__attribute__((address_space(3))) s16x4*)p));
}
__device__ __forceinline__ bf16x8 cat8(s16x4 a, s16x4 b) { return (bf16x8){a[0], a[1], a[2], a[3], b[0], b[1], b[2], b[3]}; }

__global__ __launch_bounds__(512, 2) void fused_gqa(const char* __restrict__ xA, const char* __restrict__ kA, const char* __restrict__ vA,
                                                    const char* __restrict__ wqA, const char* __restrict__ woA,
                                                    const char* __restrict__ xB, const char* __restrict__ kB, const char* __restrict__ vB,
                                                    const char* __restrict__ wqB, const char* __restrict__ woB, float* __restrict__ part) {
    __shared__ __attribute__((aligned(1024))) char lds_raw[LDS_TOTAL];
    const int wid = __builtin_amdgcn_readfirstlane(threadIdx.x >> 6);
    const int wg = blockIdx.x, xg = wg & 7, br = xg >> 2, hg = xg & 3, b = wg >> 3;
    const unsigned lds0 = (unsigned)(uintptr_t)lds_raw;
    const lds_ptr L = (lds_ptr)lds_raw;
    const char* xp = br ? xB : xA;
    const char* wq = br ? wqB : wqA;
    const char* wo = br ? woB : woA;

    {
        const int lane = lane_id();
        const unsigned voff = (unsigned)((lane & 15) * 1152 + (lane >> 4) * 16);
        const char* xb = xp + (size_t)b * 64 * 1152;
#pragma unroll
        for (int i = 0; i < 9; ++i) {
            const int blk = wid * 9 + i, s = blk >> 2, m = blk & 3;
            dma1(voff, xb + m * 16 * 1152 + s * 64, lds0 + LDS_X + blk * 1024);
        }
    }

    if (wid < 4) {
        const int lane = lane_id();
        const int aw = wid, g = aw >> 1, half = aw & 1;
        const int r = lane & 31, h = lane >> 5;
        const char* kb = (br ? kB : kA) + (size_t)(b * 16 + 4 * hg) * 131072;
        const char* vb = (br ? vB : vA) + (size_t)(b * 16 + 4 * hg) * 131072;
        const unsigned dvoff = (unsigned)((8 * aw + ((lane >> 2) & 7)) * 256 + ((lane >> 5) * 4 + ((lane & 3) ^ ((2 * aw + ((lane >> 4) & 1)) & 3))) * 16);
        const unsigned ddst = lds0 + LDS_KV + aw * 2048;
        unsigned pf0, pf1, pf2;
        {
            const int idx = aw * 64 + lane;
            const char* q0 = wq + (size_t)(18 * b) * 8192 + hg * 2048;
            const char* o0 = wo + (size_t)(1024 * hg + 32 * b) * 1152;
            const char* p0 = q0 + (idx >> 4) * 8192 + (idx & 15) * 128;
            const char* p1 = o0 + idx * 128;
            const char* p2 = (lane < 32) ? q0 + (16 + (lane >> 4)) * 8192 + (lane & 15) * 128 : o0 + (224 + lane) * 128;
            asm volatile("global_load_dword %0, %3, off\n\tglobal_load_dword %1, %4, off\n\tglobal_load_dword %2, %5, off"
                         : "=&v"(pf0), "=&v"(pf1), "=&v"(pf2) : "v"(p0), "v"(p2), "v"(p1) : "memory");
        }
#define ISSUE_K(t, slot) dma2nt(dvoff, kb + (size_t)(t) * 8192, kb + (size_t)(t) * 8192 + 128 - 1024, ddst + (slot))
#define ISSUE_V(t, slot) dma2nt(dvoff, vb + (size_t)(t) * 8192, vb + (size_t)(t) * 8192 + 128 - 1024, ddst + (slot))
        ISSUE_K(0, 0); ISSUE_V(0, 8192); ISSUE_K(1, 2 * 8192); ISSUE_V(1, 3 * 8192); ISSUE_K(2, 4 * 8192); ISSUE_V(2, 5 * 8192); ISSUE_K(3, 6 * 8192);
        WAIT_BAR(17);
        const int kbase = LDS_KV + 2048 * (r >> 3) + 64 * (r & 7) + 16 * (h ^ ((r >> 2) & 3));
        const int qq = (lane & 15) >> 2, pp = lane & 3, gam = (lane >> 4) & 1;
        const int vbase = LDS_KV + 64 * (4 * h + qq) + 16 * ((2 * gam + (pp >> 1)) ^ h) + 8 * (pp & 1);
        const int qoff = LDS_QO + (16 * g + 2 * half) * 1024 + lane * 16;
        const int ooff = LDS_QO + (16 * g + 2 * half + (r >> 4)) * 1024 + (r & 15) * 16 + 8 * h;
        f32x16 o[4];
        float lsum = 0.f;
        int ks = 0, vs = 8192;
        WAIT_BAR(10);
        asm volatile("" ::"v"(pf0), "v"(pf1), "v"(pf2));
        for (int u = 0; u < 5; ++u) {
            bf16x8 qf[8];
            if (u < 4) {
#pragma unroll
                for (int s = 0; s < 8; ++s) qf[s] = ldsr128(L + qoff + (s >> 1) * 4096 + (s & 1) * 1024);
            }
            if (u > 0) {
                auto rr = __builtin_amdgcn_permlane32_swap(__float_as_uint(lsum), __float_as_uint(lsum), false, false);
                const float inv = 1.0f / (__uint_as_float(rr[0]) + __uint_as_float(rr[1]));
                asm volatile("s_waitcnt lgkmcnt(0)" ::: "memory");
#pragma unroll
                for (int t = 0; t < 4; ++t)
#pragma unroll
                    for (int k = 0; k < 4; ++k) {
                        u32x2 w = {cvtpk(o[t][4 * k] * inv, o[t][4 * k + 1] * inv), cvtpk(o[t][4 * k + 2] * inv, o[t][4 * k + 3] * inv)};
                        *(__attribute__((address_space(3))) u32x2*)(L + ooff + t * 4096 + k * 256) = w;
                    }
            }
            LGKM_BAR();
            if (u == 4) break;
#pragma unroll
            for (int t = 0; t < 4; ++t) o[t] = f32x16{};
            lsum = 0.f;
            float mref = 0.f;
            f32x16 negm = f32x16{};
            for (int tt = 0; tt < 16; ++tt) {
                const int T = u * 16 + tt;
                if (T > 0) {
                    const int pk = (ks >= 2 * 8192) ? ks - 2 * 8192 : ks + 5 * 8192;
                    const int pv = (vs >= 2 * 8192) ? vs - 2 * 8192 : vs + 5 * 8192;
                    if (T + 2 < 64) ISSUE_V(T + 2, pk);
                    if (T + 3 < 64) ISSUE_K(T + 3, pv);
                }
                const lds_cptr kp0 = L + kbase + ks, kp1 = L + (kbase ^ 32) + ks;
                bf16x8 kf[8];
#pragma unroll
                for (int s = 0; s < 8; ++s) kf[s] = ldsr128(((s & 1) ? kp1 : kp0) + (s >> 1) * 512);
                f32x16 sc = MFMA32(kf[0], qf[0], negm);
#pragma unroll
                for (int s = 1; s < 8; ++s) sc = MFMA32(kf[s], qf[s], sc);
                const lds_cptr vp0 = L + vbase + vs, vp1 = L + (vbase ^ 32) + vs;
                s16x4 vl[8], vh[8];
#pragma unroll
                for (int i = 0; i < 8; ++i) {
                    vl[i] = vtr(vp0 + (i >> 2) * 4096 + (i & 3) * 512);
                    vh[i] = vtr(vp1 + (i >> 2) * 4096 + 2048 + (i & 3) * 512);
                }
                float a = MX3(sc[0], sc[1], sc[2]), c2 = MX3(sc[3], sc[4], sc[5]);
                a = MX3(a, sc[6], sc[7]); c2 = MX3(c2, sc[8], sc[9]); a = MX3(a, sc[10], sc[11]); c2 = MX3(c2, sc[12], sc[13]);
                a = MX3(a, sc[14], sc[15]); a = __builtin_fmaxf(a, c2);
                auto rr = __builtin_amdgcn_permlane32_swap(__float_as_uint(a), __float_as_uint(a), false, false);
                const float rm = __builtin_fmaxf(__uint_as_float(rr[0]), __uint_as_float(rr[1]));
                if (tt == 0 || __builtin_expect(__any(rm > THR), 0)) {
                    const float dl = (tt == 0) ? rm : __builtin_fmaxf(rm, 0.f);
                    const float al = (tt == 0) ? 1.0f : __builtin_amdgcn_exp2f(-dl);
                    mref += dl; lsum *= al;
#pragma unroll
                    for (int i = 0; i < 16; ++i) { sc[i] -= dl; negm[i] = -mref; }
#pragma unroll
                    for (int t = 0; t < 4; ++t)
#pragma unroll
                        for (int i = 0; i < 16; ++i) o[t][i] *= al;
                }
                float s0 = 0.f, s1 = 0.f;
#pragma unroll
                for (int i = 0; i < 16; i += 2) {
                    sc[i] = __builtin_amdgcn_exp2f(sc[i]); sc[i + 1] = __builtin_amdgcn_exp2f(sc[i + 1]);
                    s0 += sc[i]; s1 += sc[i + 1];
                }
                lsum += s0 + s1;
                const u32x4 p0 = {cvtpk(sc[0], sc[1]), cvtpk(sc[2], sc[3]), cvtpk(sc[4], sc[5]), cvtpk(sc[6], sc[7])};
                const u32x4 p1 = {cvtpk(sc[8], sc[9]), cvtpk(sc[10], sc[11]), cvtpk(sc[12], sc[13]), cvtpk(sc[14], sc[15])};
#pragma unroll
                for (int t = 0; t < 4; ++t) o[t] = MFMA32(cat8(vl[t], vh[t]), __builtin_bit_cast(bf16x8, p0), o[t]);
#pragma unroll
                for (int t = 0; t < 4; ++t) o[t] = MFMA32(cat8(vl[4 + t], vh[4 + t]), __builtin_bit_cast(bf16x8, p1), o[t]);
                ks = (ks >= 5 * 8192) ? ks - 5 * 8192 : ks + 2 * 8192;
                vs = (vs >= 5 * 8192) ? vs - 5 * 8192 : vs + 2 * 8192;
                if (T < 61) { WAIT_BAR(6); } else { WAIT_BAR(0); }
            }
        }
        __builtin_amdgcn_endpgm();
    } else {
        const int lane = lane_id();
        const int pw = wid - 4;
        const int r = lane & 31, h = lane >> 5, c = lane & 15, q = lane >> 4;
        const int qq = (lane & 15) >> 2, pp = lane & 3, gam = (lane >> 4) & 1;
        const unsigned wvoff = (unsigned)((lane >> 3) * 8192 + (((lane & 7) ^ (((lane >> 4) & 1) << 2)) * 16));
        const char* wqb = wq + hg * 2048 + pw * 128;
        const unsigned wdst = lds0 + LDS_QO + pw * 8192;
        const char* wp;
#define SET_W(u) do { wp = wqb + (u) * 512; asm volatile("" : "+s"(wp)); } while (0)
#define ISSUE_W(i) do { dma2(wvoff, wp, wp + (65536 - 1024), wdst + ((i) & 3) * 2048); wp += 131072; asm volatile("" : "+s"(wp)); } while (0)
#define START_W(u) do { SET_W(u); ISSUE_W(0); ISSUE_W(1); ISSUE_W(2); ISSUE_W(3); } while (0)
        int wbase = LDS_QO + pw * 8192 + 128 * (8 * h + qq) + 16 * (2 * gam + (pp & 1)) + 8 * (pp >> 1) + 64 * (qq >> 1); PIN(wbase);
        int xbase = LDS_X + (r >> 4) * 1024 + h * 256 + (r & 15) * 16; PIN(xbase);
        int obase = LDS_QO + lane * 16; PIN(obase);
        const int qwbase = obase + pw * 8192;
        const auto ro = __builtin_amdgcn_make_buffer_rsrc((void*)wo, 0, 4096 * 1152, 0x00020000);
        const int voa = (1024 * hg + 8 * q) * 1152 + (128 * pw + 8 * c) * 2;
        const int voc = (1024 * hg + 8 * q + (c & 1)) * 1152 + (512 + 16 * pw + (c & 14)) * 2;
        const unsigned selc = (c & 1) ? 0x03020706u : 0x05040100u;
        f32x4 acc[9][4];
        u32x4 raw[8]; unsigned rc[4];
        int so;
#define LOAD_RAW() do { _Pragma("unroll") for (int j = 0; j < 8; ++j) raw[j] = __builtin_amdgcn_raw_buffer_load_b128(ro, voa, so + j * 1152, 0); \
                        _Pragma("unroll") for (int j = 0; j < 4; ++j) rc[j] = __builtin_amdgcn_raw_buffer_load_b32(ro, voc, so + j * 2304, 0); \
                        so += 32 * 1152; asm volatile("" : "+s"(so)); } while (0)
        START_W(0);
        WAIT_BAR(0);
#define RD_FRAGS(k, buf) do { \
            af[buf][0] = cat8(vtr(L + wbase + ((k) & 3) * 2048), vtr(L + wbase + ((k) & 3) * 2048 + 512)); \
            af[buf][1] = cat8(vtr(L + (wbase ^ 64) + ((k) & 3) * 2048), vtr(L + (wbase ^ 64) + ((k) & 3) * 2048 + 512)); \
            if ((k) == 32) { xbase += 65536; PIN(xbase); } \
            bfr[buf][0] = ldsr128(L + xbase + (((k) >> 1) & 15) * 4096 + ((k) & 1) * 512); \
            bfr[buf][1] = ldsr128(L + xbase + (((k) >> 1) & 15) * 4096 + ((k) & 1) * 512 + 2048); \
            if ((k) == 35) { xbase -= 65536; PIN(xbase); } } while (0)
#define Q_PHASE(BARS) do { \
            f32x16 qa[2][2]; \
            qa[0][0] = f32x16{}; qa[0][1] = f32x16{}; qa[1][0] = f32x16{}; qa[1][1] = f32x16{}; \
            bf16x8 af[2][2], bfr[2][2]; \
            WAITVM(6); \
            RD_FRAGS(0, 0); \
            _Pragma("unroll") for (int k = 0; k < 36; ++k) { \
                if (k < 35) { \
                    if (k < 33) WAITVM(4); else if (k == 33) WAITVM(2); else WAITVM(0); \
                    RD_FRAGS(k + 1, (k + 1) & 1); \
                } \
                SBAR(); \
                qa[0][0] = MFMA32(af[k & 1][0], bfr[k & 1][0], qa[0][0]); \
                qa[0][1] = MFMA32(af[k & 1][0], bfr[k & 1][1], qa[0][1]); \
                qa[1][0] = MFMA32(af[k & 1][1], bfr[k & 1][0], qa[1][0]); \
                qa[1][1] = MFMA32(af[k & 1][1], bfr[k & 1][1], qa[1][1]); \
                PIN(qa[0][0]); PIN(qa[0][1]); PIN(qa[1][0]); PIN(qa[1][1]); \
                SBAR(); \
                if (k + 4 < 36) { asm volatile("s_waitcnt lgkmcnt(6)" ::: "memory"); ISSUE_W(k + 4); } \
                if (BARS && k < 35 && (2 * k + 2) / 9 != (2 * k) / 9) BAR(); \
            } \
            _Pragma("unroll") for (int t = 0; t < 2; ++t) \
                _Pragma("unroll") for (int m = 0; m < 2; ++m) \
                    _Pragma("unroll") for (int k = 0; k < 2; ++k) { \
                        const f32x16& a = qa[t][m]; \
                        u32x4 w = {cvtpk(a[8 * k] * QSCALE, a[8 * k + 1] * QSCALE), cvtpk(a[8 * k + 2] * QSCALE, a[8 * k + 3] * QSCALE), \
                                   cvtpk(a[8 * k + 4] * QSCALE, a[8 * k + 5] * QSCALE), cvtpk(a[8 * k + 6] * QSCALE, a[8 * k + 7] * QSCALE)}; \
                        *(__attribute__((address_space(3))) u32x4*)(L + qwbase + t * 4096 + m * 2048 + k * 1024) = w; \
                    } \
            SBAR(); \
        } while (0)
#define O_PHASE(BARS, NEXT) do { \
            bf16x8 of[4]; \
            _Pragma("unroll") for (int m = 0; m < 4; ++m) of[m] = ldsr128(L + obase + m * 1024); \
            _Pragma("unroll") for (int s = 0; s < 8; ++s) { \
                u32x4 fr[9]; \
                _Pragma("unroll") for (int t = 0; t < 8; ++t) \
                    _Pragma("unroll") for (int j = 0; j < 4; ++j) \
                        fr[t][j] = __builtin_amdgcn_perm(raw[2 * j + 1][t >> 1], raw[2 * j][t >> 1], (t & 1) ? 0x07060302u : 0x05040100u); \
                _Pragma("unroll") for (int j = 0; j < 4; ++j) fr[8][j] = __builtin_amdgcn_perm((unsigned)__builtin_amdgcn_mov_dpp((int)rc[j], 0xB1, 0xf, 0xf, true), rc[j], selc); \
                _Pragma("unroll") for (int t = 0; t < 9; ++t) PIN(fr[t]); \
                SBAR(); \
                if (s < 7) LOAD_RAW(); \
                else if ((NEXT) >= 0) START_W(NEXT); \
                SBAR(); \
                asm volatile("s_nop 1"); \
                _Pragma("unroll") for (int t = 0; t < 9; ++t) \
                    _Pragma("unroll") for (int m = 0; m < 4; ++m) MFMA16_ACC(acc[t][m], fr[t], of[m]); \
                SBAR(); \
                if (s < 7) { _Pragma("unroll") for (int m = 0; m < 4; ++m) of[m] = ldsr128(L + obase + (s + 1) * 4096 + m * 1024); } \
                if (BARS) LGKM_BAR(); \
            } \
        } while (0)
        Q_PHASE(false);
        LGKM_BAR();
        BAR();
        START_W(1);
        for (int j = 0; j < 8; ++j) BAR();
#pragma unroll
        for (int t = 0; t < 9; ++t)
#pragma unroll
            for (int m = 0; m < 4; ++m) acc[t][m] = f32x4{};
        so = 0;
        for (int i = 1; i < 4; ++i) {
            Q_PHASE(true);
            LOAD_RAW();
            LGKM_BAR();
            BAR();
            O_PHASE(true, (i < 3 ? i + 1 : -1));
        }
        for (int j = 0; j < 7; ++j) BAR();
        LOAD_RAW();
        BAR();
        BAR();
        O_PHASE(false, -1);
        asm volatile("s_nop 15\n\ts_nop 15");
        f32x4* pp4 = (f32x4*)part + ((size_t)(wg * 4 + pw) * 36) * 64 + lane_id();
#pragma unroll
        for (int t = 0; t < 9; ++t)
#pragma unroll
            for (int m = 0; m < 4; ++m) pp4[(t * 4 + m) * 64] = acc[t][m];
        __builtin_amdgcn_endpgm();
    }
}

__global__ __launch_bounds__(256) void reduce_out(const float* __restrict__ part, unsigned short* __restrict__ out) {
    const int tid = threadIdx.x, lane = tid & 63, m = tid >> 6;
    const int blk = blockIdx.x, pw = blk & 3, br = (blk >> 2) & 1, b = blk >> 3;
    const int c = lane & 15, q = lane >> 4;
    f32x4 s[9];
#pragma unroll
    for (int t = 0; t < 9; ++t) s[t] = f32x4{};
#pragma unroll
    for (int hg = 0; hg < 4; ++hg) {
        const int wg = b * 8 + br * 4 + hg;
        const f32x4* p = (const f32x4*)part + ((size_t)(wg * 4 + pw) * 36) * 64 + lane;
#pragma unroll
        for (int t = 0; t < 9; ++t) s[t] += p[(t * 4 + m) * 64];
    }
    unsigned short* orow = out + ((size_t)(b * 64 + 16 * m + c) * 1152 + br * 576);
#pragma unroll
    for (int r = 0; r < 4; ++r) {
        u32x4 w = {cvtpk(s[0][r], s[1][r]), cvtpk(s[2][r], s[3][r]), cvtpk(s[4][r], s[5][r]), cvtpk(s[6][r], s[7][r])};
        *(u32x4*)(orow + 128 * pw + 32 * q + 8 * r) = w;
    }
    u32x2 w = {cvtpk(s[8][0], s[8][1]), cvtpk(s[8][2], s[8][3])};
    *(u32x2*)(orow + 512 + 16 * pw + 4 * q) = w;
}

extern "C" void kernel_launch(void* const* d_in, const int* in_sizes, int n_in, void* d_out, int out_size, void* d_ws, size_t ws_size,
                              hipStream_t stream) {
    (void)in_sizes; (void)n_in; (void)out_size; (void)ws_size;
    float* part = (float*)d_ws;
    fused_gqa<<<256, 512, 0, stream>>>((const char*)d_in[0], (const char*)d_in[1], (const char*)d_in[2], (const char*)d_in[3], (const char*)d_in[4],
                                       (const char*)d_in[5], (const char*)d_in[6], (const char*)d_in[7], (const char*)d_in[8], (const char*)d_in[9], part);
    reduce_out<<<256, 256, 0, stream>>>(part, (unsigned short*)d_out);
}
```

```cpp
#include <hip/hip_runtime.h>
#include <hip/hip_bf16.h>
#include <stdint.h>

typedef short bf16x8 __attribute__((ext_vector_type(8)));
typedef short s16x4 __attribute__((ext_vector_type(4)));
typedef float f32x16 __attribute__((ext_vector_type(16)));
typedef float f32x4 __attribute__((ext_vector_type(4)));
typedef float f32x2 __attribute__((ext_vector_type(2)));
typedef unsigned u32x4 __attribute__((ext_vector_type(4)));
typedef unsigned u32x2 __attribute__((ext_vector_type(2)));
typedef __attribute__((address_space(3))) char* lds_ptr;
typedef __attribute__((address_space(3))) const char* lds_cptr;

constexpr int LDS_X = 0, LDS_QO = 73728, LDS_KV = 106496, LDS_TOTAL = 163840;
constexpr float QSCALE = 0.08838834764831845f * 1.4426950408889634f;
constexpr float THR = 8.0f;
#define LSTEP 2

#define SBAR() __builtin_amdgcn_sched_barrier(0)
#define PIN(x) asm volatile("" : "+v"(x))
#define MFMA32(a, b, c) __builtin_amdgcn_mfma_f32_32x32x16_bf16(a, b, c, 0, 0, 0)
#define MFMA16(a, b, c) __builtin_amdgcn_mfma_f32_16x16x32_bf16(a, b, c, 0, 0, 0)
#define MFMA16_ACC(c, a, b) asm volatile("v_mfma_f32_16x16x32_bf16 %0, %1, %2, %0" : "+v"(c) : "v"(a), "v"(b))
#define PRE()
#define POST()
#define PROF_DECL
#define PROF_END()
#define WAIT_BAR(N) do { PRE(); asm volatile("s_waitcnt vmcnt(" #N ") lgkmcnt(0)\n\ts_barrier" ::: "memory"); POST(); } while (0)
#define LGKM_BAR() do { PRE(); asm volatile("s_waitcnt lgkmcnt(0)\n\ts_barrier" ::: "memory"); POST(); } while (0)
#define BAR() do { PRE(); asm volatile("s_barrier" ::: "memory"); POST(); } while (0)
#define MARK() do { PRE(); POST(); } while (0)
#define WAITVM(N) asm volatile("s_waitcnt vmcnt(" #N ")" ::: "memory")
#define MX3(a, b, c) __builtin_fmaxf(__builtin_fmaxf((a), (b)), (c))

__device__ __forceinline__ unsigned cvtpk(float lo, float hi) {
    typedef float f32x2_t __attribute__((ext_vector_type(2)));
    typedef __bf16 bf16x2_t __attribute__((ext_vector_type(2)));
    return __builtin_bit_cast(unsigned, __builtin_convertvector((f32x2_t){lo, hi}, bf16x2_t));
}
__device__ __forceinline__ void dma1(unsigned voff, const char* sbase, unsigned lds_base) {
    unsigned sv;
    asm volatile("s_mov_b32 %0, m0\n\ts_mov_b32 m0, %3\n\ts_nop 0\n\tglobal_load_lds_dwordx4 %1, %2\n\ts_mov_b32 m0, %0"
                 : "=&s"(sv) : "v"(voff), "s"(sbase), "s"(lds_base) : "memory");
}
__device__ __forceinline__ void dma2(unsigned voff, const char* s0, const char* s1m, unsigned lds_base) {
    unsigned sv;
    asm volatile("s_mov_b32 %0, m0\n\ts_mov_b32 m0, %4\n\ts_nop 0\n\tglobal_load_lds_dwordx4 %1, %2\n\tglobal_load_lds_dwordx4 %1, %3 offset:1024\n\ts_mov_b32 m0, %0"
                 : "=&s"(sv) : "v"(voff), "s"(s0), "s"(s1m), "s"(lds_base) : "memory");
}
__device__ __forceinline__ void dma2nt(unsigned voff, const char* s0, const char* s1m, unsigned lds_base) {
    unsigned sv;
    asm volatile("s_mov_b32 %0, m0\n\ts_mov_b32 m0, %4\n\ts_nop 0\n\tglobal_load_lds_dwordx4 %1, %2 nt\n\tglobal_load_lds_dwordx4 %1, %3 offset:1024 nt\n\ts_mov_b32 m0, %0"
                 : "=&s"(sv) : "v"(voff), "s"(s0), "s"(s1m), "s"(lds_base) : "memory");
}
__device__ __forceinline__ int lane_id() { int l; asm volatile("v_mbcnt_lo_u32_b32 %0, -1, 0\n\tv_mbcnt_hi_u32_b32 %0, -1, %0" : "=v"(l)); return l; }
__device__ __forceinline__ bf16x8 ldsr128(lds_cptr p) { return *(const __attribute__((address_space(3))) bf16x8*)p; }
__device__ __forceinline__ s16x4 vtr(lds_cptr p) {
    return __builtin_bit_cast(s16x4, __builtin_amdgcn_ds_read_tr16_b64_v4i16((__attribute__((address_space(3))) s16x4*)p));
}
__device__ __forceinline__ bf16x8 cat8(s16x4 a, s16x4 b) { return (bf16x8){a[0], a[1], a[2], a[3], b[0], b[1], b[2], b[3]}; }

__global__ __launch_bounds__(512, 2) void fused_gqa(const char* __restrict__ xA, const char* __restrict__ kA, const char* __restrict__ vA,
                                                    const char* __restrict__ wqA, const char* __restrict__ woA,
                                                    const char* __restrict__ xB, const char* __restrict__ kB, const char* __restrict__ vB,
                                                    const char* __restrict__ wqB, const char* __restrict__ woB, float* __restrict__ part) {
    __shared__ __attribute__((aligned(1024))) char lds_raw[LDS_TOTAL];
    const int wid = __builtin_amdgcn_readfirstlane(threadIdx.x >> 6);
    const int wg = blockIdx.x, xg = wg & 7, br = xg >> 2, hg = xg & 3, b = wg >> 3;
    const unsigned lds0 = (unsigned)(uintptr_t)lds_raw;
    const lds_ptr L = (lds_ptr)lds_raw;
    PROF_DECL
    PRE(); POST();
    const char* xp = br ? xB : xA;
    const char* wq = br ? wqB : wqA;
    const char* wo = br ? woB : woA;

    unsigned pf0 = 0;
    if (wid < 4) {
        const int idx = wid * 18 + (lane_id() & 31);
        const int id2 = idx > 71 ? 71 : idx;
        const char* p0 = wq + (size_t)(18 * b + (id2 >> 2)) * 8192 + hg * 2048 + (id2 & 3) * 128;
        asm volatile("global_load_dword %0, %1, off" : "=&v"(pf0) : "v"(p0) : "memory");
    }
    {
        const int lane = lane_id();
        const unsigned voff = (unsigned)((lane & 15) * 1152 + (lane >> 4) * 16);
        const char* xb = xp + (size_t)b * 64 * 1152;
#pragma unroll
        for (int i = 0; i < 9; ++i) {
            const int blk = wid * 9 + i, s = blk >> 2, m = blk & 3;
            dma1(voff, xb + m * 16 * 1152 + s * 64, lds0 + LDS_X + blk * 1024);
        }
    }

    MARK();
    if (wid < 4) {
        const int lane = lane_id();
        const int aw = wid, g = aw >> 1, half = aw & 1;
        const int r = lane & 31, h = lane >> 5;
        const char* kb = (br ? kB : kA) + (size_t)(b * 16 + 4 * hg) * 131072;
        const char* vb = (br ? vB : vA) + (size_t)(b * 16 + 4 * hg) * 131072;
        const unsigned dvoff = (unsigned)((8 * aw + ((lane >> 2) & 7)) * 256 + ((lane >> 5) * 4 + ((lane & 3) ^ ((2 * aw + ((lane >> 4) & 1)) & 3))) * 16);
        const unsigned ddst = lds0 + LDS_KV + aw * 2048;
#define ISSUE(src, slot) dma2nt(dvoff, (src), (src) + 128 - 1024, ddst + (slot))
        WAIT_BAR(0);
        ISSUE(kb, 0); ISSUE(kb + 8192, 8192); ISSUE(vb, 32768); ISSUE(kb + 16384, 16384); ISSUE(vb + 8192, 40960); ISSUE(kb + 24576, 24576); ISSUE(vb + 16384, 49152);
        asm volatile("" ::"v"(pf0));
        const int kbase = LDS_KV + 2048 * (r >> 3) + 64 * (r & 7) + 16 * (h ^ ((r >> 2) & 3));
        const int qq = (lane & 15) >> 2, pp = lane & 3, gam = (lane >> 4) & 1;
        const int vbase = LDS_KV + 32768 + 64 * (4 * h + qq) + 16 * ((2 * gam + (pp >> 1)) ^ h) + 8 * (pp & 1);
        const int qoff = LDS_QO + (16 * g + 2 * half) * 1024 + lane * 16;
        const int ooff = LDS_QO + (16 * g + 2 * half + (r >> 4)) * 1024 + (r & 15) * 16 + 8 * h;
        f32x16 o[4], sA, sB = f32x16{}, negm = f32x16{};
        bf16x8 qf[8], kf[8];
        f32x2 ls2 = f32x2{};
        float mref = 0.f;
        int t = 0, vr = 16384;
#pragma unroll
        for (int i = 0; i < 4; ++i) o[i] = f32x16{};
        WAIT_BAR(10);
#pragma unroll
        for (int s = 0; s < 8; ++s) kf[s] = ldsr128(L + (kbase ^ ((s & 1) * 32)) + (s >> 1) * 512);
#define VRD(i) do { vl[i] = vtr(vp0 + ((i) >> 2) * 4096 + ((i) & 3) * 512); vh[i] = vtr(vp1 + ((i) >> 2) * 4096 + 2048 + ((i) & 3) * 512); } while (0)
#define KRD(s) kf[s] = ldsr128((((s) & 1) ? kp1 : kp0) + ((s) >> 1) * 512)
#define EXP2(X, i) do { float e0_ = __builtin_amdgcn_exp2f(X[i]), e1_ = __builtin_amdgcn_exp2f(X[(i) + 1]); PIN(e0_); PIN(e1_); X[i] = e0_; X[(i) + 1] = e1_; } while (0)
#define SUM2(X, i) ls2 += (f32x2){X[i], X[(i) + 1]}
#define QK(C, s) do { SBAR(); C = MFMA32(kf[s], qf[s], C); SBAR(); } while (0)
#define PV(i, p) do { SBAR(); o[(i) & 3] = MFMA32(cat8(vl[i], vh[i]), __builtin_bit_cast(bf16x8, p), o[(i) & 3]); SBAR(); } while (0)
#define STEP(C, P, BOUND) do { \
            s16x4 vl[8], vh[8]; \
            const lds_cptr vp0 = L + vbase + vr, vp1 = L + (vbase ^ 32) + vr; \
            const int kr = ((t + 1) & 3) * 8192; \
            const lds_cptr kp0 = L + kbase + kr, kp1 = L + (kbase ^ 32) + kr; \
            if (BOUND) { _Pragma("unroll") for (int s = 0; s < 8; ++s) qf[s] = ldsr128(L + qoff + (s >> 1) * 4096 + (s & 1) * 1024); C = f32x16{}; QK(C, 0); } \
            else { SBAR(); asm volatile("v_mfma_f32_32x32x16_bf16 %0, %2, %3, %1" : "=&v"(C), "+v"(negm) : "v"(kf[0]), "v"(qf[0])); SBAR(); } \
            VRD(0); VRD(1); EXP2(P, 8); \
            QK(C, 1); VRD(2); VRD(3); EXP2(P, 10); \
            QK(C, 2); VRD(4); VRD(5); EXP2(P, 12); \
            QK(C, 3); VRD(6); VRD(7); EXP2(P, 14); \
            QK(C, 4); \
            if (!(BOUND) || (u > 0 && u < 4)) { \
                const int tv = t + 1 > 63 ? 63 : t + 1; \
                const char* sp = (t == 1) ? kb + 4 * 8192 : vb + (size_t)tv * 8192; \
                const int sd = (t == 1) ? 0 : 32768 + (vr == 0 ? 16384 : vr - 8192); \
                ISSUE(sp, sd); } \
            u32x4 p0 = {cvtpk(P[0], P[1]), cvtpk(P[2], P[3]), cvtpk(P[4], P[5]), cvtpk(P[6], P[7])}; PIN(p0); \
            QK(C, 5); SUM2(P, 0); SUM2(P, 2); SUM2(P, 4); SUM2(P, 6); PIN(ls2); \
            QK(C, 6); \
            u32x4 p1 = {cvtpk(P[8], P[9]), cvtpk(P[10], P[11]), cvtpk(P[12], P[13]), cvtpk(P[14], P[15])}; PIN(p1); \
            QK(C, 7); \
            if (!(BOUND) || (u > 0 && u < 4)) { const int tk = t + 4 > 63 ? 63 : t + 4; const char* sp = kb + (size_t)tk * 8192; ISSUE(sp, (t & 3) * 8192); } \
            SUM2(P, 8); SUM2(P, 10); SUM2(P, 12); SUM2(P, 14); PIN(ls2); \
            PV(0, p0); KRD(0); KRD(1); \
            PV(1, p0); KRD(2); KRD(3); \
            float a_ = MX3(C[0], C[1], C[2]), c_ = MX3(C[3], C[4], C[5]); \
            a_ = MX3(a_, C[6], C[7]); c_ = MX3(c_, C[8], C[9]); a_ = MX3(a_, C[10], C[11]); c_ = MX3(c_, C[12], C[13]); a_ = MX3(a_, C[14], C[15]); PIN(a_); PIN(c_); \
            PV(2, p0); KRD(4); KRD(5); \
            asm volatile("v_max_f32 %0, %0, %1" : "+v"(a_) : "v"(c_)); \
            auto rr_ = __builtin_amdgcn_permlane32_swap(__float_as_uint(a_), __float_as_uint(a_), false, false); \
            float rm; asm volatile("v_max_f32 %0, %1, %2" : "=v"(rm) : "v"(rr_[0]), "v"(rr_[1])); \
            PV(3, p0); KRD(6); KRD(7); \
            float al = 1.0f, lprev = 0.f; bool need = false; \
            if (BOUND) { \
                lprev = ls2[0] + ls2[1]; ls2 = f32x2{}; mref = rm; \
                _Pragma("unroll") for (int i = 0; i < 16; ++i) { C[i] -= rm; negm[i] = -rm; } \
                PIN(negm); \
            } else { \
                need = __builtin_amdgcn_ballot_w64(rm > THR) != 0; \
                if (__builtin_expect(need, 0)) { \
                    const float dl = __builtin_fmaxf(rm, 0.f); \
                    al = __builtin_amdgcn_exp2f(-dl); mref += dl; ls2 *= al; \
                    _Pragma("unroll") for (int i = 0; i < 16; ++i) { C[i] -= dl; negm[i] = -mref; } \
                    PIN(negm); \
                } \
            } \
            PV(4, p1); EXP2(C, 0); \
            PV(5, p1); EXP2(C, 2); \
            PV(6, p1); EXP2(C, 4); \
            PV(7, p1); EXP2(C, 6); \
            SBAR(); \
            if (BOUND) { \
                if (u > 0) { \
                    auto r2_ = __builtin_amdgcn_permlane32_swap(__float_as_uint(lprev), __float_as_uint(lprev), false, false); \
                    const float inv = 1.0f / (__uint_as_float(r2_[0]) + __uint_as_float(r2_[1])); \
                    _Pragma("unroll") for (int tt = 0; tt < 4; ++tt) \
                        _Pragma("unroll") for (int k = 0; k < 4; ++k) { \
                            u32x2 w = {cvtpk(o[tt][4 * k] * inv, o[tt][4 * k + 1] * inv), cvtpk(o[tt][4 * k + 2] * inv, o[tt][4 * k + 3] * inv)}; \
                            *(__attribute__((address_space(3))) u32x2*)(L + ooff + tt * 4096 + k * 256) = w; \
                        } \
                } \
                _Pragma("unroll") for (int i = 0; i < 4; ++i) o[i] = f32x16{}; \
            } else if (__builtin_expect(need, 0)) { \
                _Pragma("unroll") for (int tt = 0; tt < 4; ++tt) \
                    _Pragma("unroll") for (int i = 0; i < 16; ++i) o[tt][i] *= al; \
            } \
            ++t; vr = (vr == 16384) ? 0 : vr + 8192; \
            WAIT_BAR(6); \
        } while (0)
        for (int u = 0; u < 4; ++u) {
            STEP(sA, sB, true);
            STEP(sB, sA, false);
            for (int j = 0; j < 7; ++j) {
                STEP(sA, sB, false);
                STEP(sB, sA, false);
            }
        }
        { const int u = 4; STEP(sA, sB, true); }
        WAITVM(0);
        PROF_END();
        __builtin_amdgcn_endpgm();
    } else {
        const int lane = lane_id();
        const int pw = wid - 4;
        const int r = lane & 31, h = lane >> 5, c = lane & 15, q = lane >> 4;
        const int qq = (lane & 15) >> 2, pp = lane & 3, gam = (lane >> 4) & 1;
        const unsigned wvoff = (unsigned)((lane >> 3) * 8192 + (((lane & 7) ^ (((lane >> 4) & 1) << 2)) * 16));
        const char* wqb = wq + hg * 2048 + pw * 128;
        const unsigned wdst = lds0 + LDS_QO + pw * 8192;
        const char* wp;
#define SET_W(u) do { wp = wqb + (u) * 512; asm volatile("" : "+s"(wp)); } while (0)
#define ISSUE_W(i) do { dma2(wvoff, wp, wp + (65536 - 1024), wdst + ((i) & 3) * 2048); wp += 131072; asm volatile("" : "+s"(wp)); } while (0)
#define START_W(u) do { SET_W(u); ISSUE_W(0); ISSUE_W(1); ISSUE_W(2); ISSUE_W(3); } while (0)
        int wbase = LDS_QO + pw * 8192 + 128 * (8 * h + qq) + 16 * (2 * gam + (pp & 1)) + 8 * (pp >> 1) + 64 * (qq >> 1); PIN(wbase);
        int xbase = LDS_X + (r >> 4) * 1024 + h * 256 + (r & 15) * 16; PIN(xbase);
        int obase = LDS_QO + lane * 16; PIN(obase);
        const int qwbase = obase + pw * 8192;
        const auto ro = __builtin_amdgcn_make_buffer_rsrc((void*)wo, 0, 4096 * 1152, 0x00020000);
        const int voa = (1024 * hg + 8 * q) * 1152 + (128 * pw + 8 * c) * 2;
        const int voc = (1024 * hg + 8 * q + (c & 1)) * 1152 + (512 + 16 * pw + (c & 14)) * 2;
        const unsigned selc = (c & 1) ? 0x03020706u : 0x05040100u;
        f32x4 acc[9][4];
        u32x4 raw[8]; unsigned rc[4];
        int so;
#define LOAD_RAW() do { _Pragma("unroll") for (int j = 0; j < 8; ++j) raw[j] = __builtin_amdgcn_raw_buffer_load_b128(ro, voa, so + j * 1152, 0); \
                        _Pragma("unroll") for (int j = 0; j < 4; ++j) rc[j] = __builtin_amdgcn_raw_buffer_load_b32(ro, voc, so + j * 2304, 0); \
                        so += 32 * 1152; asm volatile("" : "+s"(so)); } while (0)
        MARK();
        START_W(0);
        WAIT_BAR(0);
#define RD_FRAGS(k, buf) do { \
            af[buf][0] = cat8(vtr(L + wbase + ((k) & 3) * 2048), vtr(L + wbase + ((k) & 3) * 2048 + 512)); \
            af[buf][1] = cat8(vtr(L + (wbase ^ 64) + ((k) & 3) * 2048), vtr(L + (wbase ^ 64) + ((k) & 3) * 2048 + 512)); \
            if ((k) == 32) { xbase += 65536; PIN(xbase); } \
            bfr[buf][0] = ldsr128(L + xbase + (((k) >> 1) & 15) * 4096 + ((k) & 1) * 512); \
            bfr[buf][1] = ldsr128(L + xbase + (((k) >> 1) & 15) * 4096 + ((k) & 1) * 512 + 2048); \
            if ((k) == 35) { xbase -= 65536; PIN(xbase); } } while (0)
#define Q_PHASE(BARS) do { \
            f32x16 qa[2][2]; \
            qa[0][0] = f32x16{}; qa[0][1] = f32x16{}; qa[1][0] = f32x16{}; qa[1][1] = f32x16{}; \
            bf16x8 af[2][2], bfr[2][2]; \
            WAITVM(6); \
            RD_FRAGS(0, 0); \
            asm volatile("s_waitcnt lgkmcnt(0)" ::: "memory"); ISSUE_W(4); \
            _Pragma("unroll") for (int k = 0; k < 36; ++k) { \
                if (k < 35) { \
                    if (k < 32) WAITVM(6); else if (k == 32) WAITVM(4); else if (k == 33) WAITVM(2); else WAITVM(0); \
                    RD_FRAGS(k + 1, (k + 1) & 1); \
                } \
                SBAR(); \
                qa[0][0] = MFMA32(af[k & 1][0], bfr[k & 1][0], qa[0][0]); \
                qa[0][1] = MFMA32(af[k & 1][0], bfr[k & 1][1], qa[0][1]); \
                qa[1][0] = MFMA32(af[k & 1][1], bfr[k & 1][0], qa[1][0]); \
                qa[1][1] = MFMA32(af[k & 1][1], bfr[k & 1][1], qa[1][1]); \
                PIN(qa[0][0]); PIN(qa[0][1]); PIN(qa[1][0]); PIN(qa[1][1]); \
                SBAR(); \
                if (k + 5 < 36) { asm volatile("s_waitcnt lgkmcnt(0)" ::: "memory"); ISSUE_W(k + 5); } \
                if (BARS && k <= 30 && k % 5 == 0) BAR(); \
            } \
            _Pragma("unroll") for (int t = 0; t < 2; ++t) \
                _Pragma("unroll") for (int m = 0; m < 2; ++m) \
                    _Pragma("unroll") for (int k = 0; k < 2; ++k) { \
                        const f32x16& a = qa[t][m]; \
                        u32x4 w = {cvtpk(a[8 * k] * QSCALE, a[8 * k + 1] * QSCALE), cvtpk(a[8 * k + 2] * QSCALE, a[8 * k + 3] * QSCALE), \
                                   cvtpk(a[8 * k + 4] * QSCALE, a[8 * k + 5] * QSCALE), cvtpk(a[8 * k + 6] * QSCALE, a[8 * k + 7] * QSCALE)}; \
                        *(__attribute__((address_space(3))) u32x4*)(L + qwbase + t * 4096 + m * 2048 + k * 1024) = w; \
                    } \
            SBAR(); \
        } while (0)
#define O_PHASE(BARS, NEXT) do { \
            bf16x8 of[4]; \
            _Pragma("unroll") for (int s = 0; s < 8; ++s) { \
                u32x4 fr[9]; \
                _Pragma("unroll") for (int t = 0; t < 8; ++t) \
                    _Pragma("unroll") for (int j = 0; j < 4; ++j) \
                        fr[t][j] = __builtin_amdgcn_perm(raw[2 * j + 1][t >> 1], raw[2 * j][t >> 1], (t & 1) ? 0x07060302u : 0x05040100u); \
                _Pragma("unroll") for (int j = 0; j < 4; ++j) fr[8][j] = __builtin_amdgcn_perm((unsigned)__builtin_amdgcn_mov_dpp((int)rc[j], 0xB1, 0xf, 0xf, true), rc[j], selc); \
                _Pragma("unroll") for (int t = 0; t < 9; ++t) PIN(fr[t]); \
                SBAR(); \
                if (s == 0) { \
                    LOAD_RAW(); SBAR(); BAR(); \
                    _Pragma("unroll") for (int m = 0; m < 4; ++m) of[m] = ldsr128(L + obase + m * 1024); \
                } \
                if (s == 7 && (NEXT) >= 0) START_W(NEXT); \
                SBAR(); \
                asm volatile("s_nop 1"); \
                _Pragma("unroll") for (int t = 0; t < 9; ++t) \
                    _Pragma("unroll") for (int m = 0; m < 4; ++m) { \
                        MFMA16_ACC(acc[t][m], fr[t], of[m]); \
                        const int j = (t * 4 + m) / LSTEP; \
                        if (s > 0 && s < 7 && (t * 4 + m) % LSTEP == 0 && j < 12) { \
                            SBAR(); \
                            if (j < 8) raw[j] = __builtin_amdgcn_raw_buffer_load_b128(ro, voa, so + j * 1152, 0); \
                            else rc[j - 8] = __builtin_amdgcn_raw_buffer_load_b32(ro, voc, so + (j - 8) * 2304, 0); \
                            SBAR(); \
                        } \
                    } \
                if (s > 0 && s < 7) { so += 32 * 1152; asm volatile("" : "+s"(so)); } \
                SBAR(); \
                if (s < 7) { _Pragma("unroll") for (int m = 0; m < 4; ++m) of[m] = ldsr128(L + obase + (s + 1) * 4096 + m * 1024); } \
                if (BARS && s < 7) LGKM_BAR(); \
            } \
        } while (0)
        Q_PHASE(false);
        LGKM_BAR();
        BAR();
        {
            const int idx = pw * 64 + lane_id();
            const char* q0 = wq + (size_t)(18 * b) * 8192 + hg * 2048 + 512;
            const char* o0 = wo + (size_t)(1024 * hg + 32 * b) * 1152;
            const int i12 = idx / 12;
            const char* p0 = idx < 216 ? q0 + i12 * 8192 + (idx - 12 * i12) * 128 : o0 + (idx & 31) * 128;
            const char* p1 = o0 + 4096 + idx * 128;
            unsigned pf1, pf2;
            asm volatile("global_load_dword %0, %2, off\n\tglobal_load_dword %1, %3, off" : "=&v"(pf1), "=&v"(pf2) : "v"(p0), "v"(p1) : "memory");
            for (int j = 0; j < 6; ++j) BAR();
            WAITVM(0);
            asm volatile("" ::"v"(pf1), "v"(pf2));
        }
        START_W(1);
        BAR();
#pragma unroll
        for (int t = 0; t < 9; ++t)
#pragma unroll
            for (int m = 0; m < 4; ++m) acc[t][m] = f32x4{};
        so = 0;
        for (int i = 1; i < 4; ++i) {
            Q_PHASE(true);
            LOAD_RAW();
            LGKM_BAR();
            O_PHASE(true, (i < 3 ? i + 1 : -1));
        }
        LOAD_RAW();
        for (int j = 0; j < 8; ++j) BAR();
        O_PHASE(false, -1);
        asm volatile("s_nop 15\n\ts_nop 15");
        f32x4* pp4 = (f32x4*)part + ((size_t)(wg * 4 + pw) * 36) * 64 + lane_id();
#pragma unroll
        for (int t = 0; t < 9; ++t)
#pragma unroll
            for (int m = 0; m < 4; ++m) pp4[(t * 4 + m) * 64] = acc[t][m];
        PROF_END();
        __builtin_amdgcn_endpgm();
    }
}

__global__ __launch_bounds__(256) void reduce_out(const float* __restrict__ part, unsigned short* __restrict__ out) {
    const int tid = threadIdx.x, lane = tid & 63, m = tid >> 6;
    const int blk = blockIdx.x, pw = blk & 3, br = (blk >> 2) & 1, b = blk >> 3;
    const int c = lane & 15, q = lane >> 4;
    f32x4 s[9];
#pragma unroll
    for (int t = 0; t < 9; ++t) s[t] = f32x4{};
#pragma unroll
    for (int hg = 0; hg < 4; ++hg) {
        const int wg = b * 8 + br * 4 + hg;
        const f32x4* p = (const f32x4*)part + ((size_t)(wg * 4 + pw) * 36) * 64 + lane;
#pragma unroll
        for (int t = 0; t < 9; ++t) s[t] += p[(t * 4 + m) * 64];
    }
    unsigned short* orow = out + ((size_t)(b * 64 + 16 * m + c) * 1152 + br * 576);
#pragma unroll
    for (int r = 0; r < 4; ++r) {
        u32x4 w = {cvtpk(s[0][r], s[1][r]), cvtpk(s[2][r], s[3][r]), cvtpk(s[4][r], s[5][r]), cvtpk(s[6][r], s[7][r])};
        *(u32x4*)(orow + 128 * pw + 32 * q + 8 * r) = w;
    }
    u32x2 w = {cvtpk(s[8][0], s[8][1]), cvtpk(s[8][2], s[8][3])};
    *(u32x2*)(orow + 512 + 16 * pw + 4 * q) = w;
}

extern "C" void kernel_launch(void* const* d_in, const int* in_sizes, int n_in, void* d_out, int out_size, void* d_ws, size_t ws_size,
                              hipStream_t stream) {
    (void)in_sizes; (void)n_in; (void)out_size; (void)ws_size;
    float* part = (float*)d_ws;
    fused_gqa<<<256, 512, 0, stream>>>((const char*)d_in[0], (const char*)d_in[1], (const char*)d_in[2], (const char*)d_in[3], (const char*)d_in[4],
                                       (const char*)d_in[5], (const char*)d_in[6], (const char*)d_in[7], (const char*)d_in[8], (const char*)d_in[9], part);
    reduce_out<<<256, 256, 0, stream>>>(part, (unsigned short*)d_out);
}
```

```cpp
#include <hip/hip_runtime.h>
#include <hip/hip_bf16.h>
#include <stdint.h>

typedef short bf16x8 __attribute__((ext_vector_type(8)));
typedef short s16x4 __attribute__((ext_vector_type(4)));
typedef float f32x16 __attribute__((ext_vector_type(16)));
typedef float f32x4 __attribute__((ext_vector_type(4)));
typedef float f32x2 __attribute__((ext_vector_type(2)));
typedef unsigned u32x4 __attribute__((ext_vector_type(4)));
typedef unsigned u32x2 __attribute__((ext_vector_type(2)));
typedef __attribute__((address_space(3))) char* lds_ptr;
typedef __attribute__((address_space(3))) const char* lds_cptr;

constexpr int LDS_X = 0, LDS_QO = 73728, LDS_KV = 106496, LDS_TOTAL = 163840;
constexpr float QSCALE = 0.08838834764831845f * 1.4426950408889634f;
constexpr float THR = 8.0f;
#define LSTEP 2
#define QP 0
#define PPOS 0
#define PACE(n) do { if ((n) > 0) asm volatile("s_nop %0" ::"n"((n) > 16 ? 15 : (n) - 1)); if ((n) > 16) asm volatile("s_nop %0" ::"n"((n) > 32 ? 15 : (n) - 17)); if ((n) > 32) asm volatile("s_nop %0" ::"n"((n) - 33)); } while (0)

#define SBAR() __builtin_amdgcn_sched_barrier(0)
#define PIN(x) asm volatile("" : "+v"(x))
#define MFMA32(a, b, c) __builtin_amdgcn_mfma_f32_32x32x16_bf16(a, b, c, 0, 0, 0)
#define MFMA16(a, b, c) __builtin_amdgcn_mfma_f32_16x16x32_bf16(a, b, c, 0, 0, 0)
#define MFMA16_ACC(c, a, b) asm volatile("v_mfma_f32_16x16x32_bf16 %0, %1, %2, %0" : "+v"(c) : "v"(a), "v"(b))
#define PRE()
#define POST()
#define PROF_DECL
#define PROF_END()
#define WAITC(vm, lg) __builtin_amdgcn_s_waitcnt(((vm) & 15) | 0x70 | ((lg) << 8) | (((vm) >> 4) << 14))
#define WAIT_BAR(N) do { PRE(); asm volatile("" ::: "memory"); WAITC(N, 0); asm volatile("s_barrier" ::: "memory"); POST(); } while (0)
#define LGKM_BAR() WAIT_BAR(63)
#define LGKM0() do { asm volatile("" ::: "memory"); WAITC(63, 0); asm volatile("" ::: "memory"); } while (0)
#define BAR() do { PRE(); asm volatile("s_barrier" ::: "memory"); POST(); } while (0)
#define MARK() do { PRE(); POST(); } while (0)
#define WAITVM(N) asm volatile("s_waitcnt vmcnt(" #N ")" ::: "memory")
#define MX3(a, b, c) __builtin_fmaxf(__builtin_fmaxf((a), (b)), (c))

__device__ __forceinline__ unsigned cvtpk(float lo, float hi) {
    typedef float f32x2_t __attribute__((ext_vector_type(2)));
    typedef __bf16 bf16x2_t __attribute__((ext_vector_type(2)));
    return __builtin_bit_cast(unsigned, __builtin_convertvector((f32x2_t){lo, hi}, bf16x2_t));
}
__device__ __forceinline__ void dma1(unsigned voff, const char* sbase, unsigned lds_base) {
    unsigned sv;
    asm volatile("s_mov_b32 %0, m0\n\ts_mov_b32 m0, %3\n\ts_nop 0\n\tglobal_load_lds_dwordx4 %1, %2\n\ts_mov_b32 m0, %0"
                 : "=&s"(sv) : "v"(voff), "s"(sbase), "s"(lds_base) : "memory");
}
#define SETM0(v) asm volatile("s_mov_b32 m0, %0" ::"s"(v) : "memory")
#define SETM0_ADD(v, imm) asm volatile("s_add_u32 m0, %0, %1" ::"s"(v), "n"(imm) : "memory", "scc")
#define BDMA2NT(voff, rs) asm volatile("buffer_load_dwordx4 %0, %1, 0 offen nt lds\n\tbuffer_load_dwordx4 %0, %1, 0 offen offset:1024 nt lds" ::"v"(voff), "s"(rs) : "memory")
#define BDMA2(v0, v1, rs, so) asm volatile("buffer_load_dwordx4 %0, %2, %3 offen lds\n\tbuffer_load_dwordx4 %1, %2, %3 offen offset:1024 lds" ::"v"(v0), "v"(v1), "s"(rs), "s"(so) : "memory")
__device__ __forceinline__ u32x4 mkrsrc(const char* p, unsigned n) {
    const unsigned long long a = (unsigned long long)(uintptr_t)p;
    return (u32x4){(unsigned)a, (unsigned)(a >> 32) & 0xffffu, n, 0x00020000u};
}
__device__ __forceinline__ int lane_id() { int l; asm volatile("v_mbcnt_lo_u32_b32 %0, -1, 0\n\tv_mbcnt_hi_u32_b32 %0, -1, %0" : "=v"(l)); return l; }
__device__ __forceinline__ bf16x8 ldsr128(lds_cptr p) { return *(const __attribute__((address_space(3))) bf16x8*)p; }
__device__ __forceinline__ s16x4 vtr(lds_cptr p) {
    return __builtin_bit_cast(s16x4, __builtin_amdgcn_ds_read_tr16_b64_v4i16((__attribute__((address_space(3))) s16x4*)p));
}
__device__ __forceinline__ bf16x8 cat8(s16x4 a, s16x4 b) { return (bf16x8){a[0], a[1], a[2], a[3], b[0], b[1], b[2], b[3]}; }

__global__ __launch_bounds__(512, 2) void fused_gqa(const char* __restrict__ xA, const char* __restrict__ kA, const char* __restrict__ vA,
                                                    const char* __restrict__ wqA, const char* __restrict__ woA,
                                                    const char* __restrict__ xB, const char* __restrict__ kB, const char* __restrict__ vB,
                                                    const char* __restrict__ wqB, const char* __restrict__ woB, float* __restrict__ part) {
    __shared__ __attribute__((aligned(1024))) char lds_raw[LDS_TOTAL];
    const int wid = __builtin_amdgcn_readfirstlane(threadIdx.x >> 6);
    const int wg = blockIdx.x, xg = wg & 7, br = xg >> 2, hg = xg & 3, b = wg >> 3;
    const unsigned lds0 = (unsigned)(uintptr_t)lds_raw;
    const lds_ptr L = (lds_ptr)lds_raw;
    PROF_DECL
    PRE(); POST();
    const char* xp = br ? xB : xA;
    const char* wq = br ? wqB : wqA;
    const char* wo = br ? woB : woA;

#define LOAD_X() do { \
        const int lane_ = lane_id(); \
        const unsigned voff = (unsigned)((lane_ & 15) * 1152 + (lane_ >> 4) * 16); \
        const char* xb = xp + (size_t)b * 64 * 1152; \
        _Pragma("unroll") for (int i = 0; i < 9; ++i) { \
            const int blk = wid * 9 + i, s = blk >> 2, m = blk & 3; \
            dma1(voff, xb + m * 16 * 1152 + s * 64, lds0 + LDS_X + blk * 1024); \
        } } while (0)
    if (wid < 4) {
        unsigned pf0;
        {
            const int idx = wid * 18 + (lane_id() & 31);
            const int id2 = idx > 71 ? 71 : idx;
            const char* p0 = wq + (size_t)(18 * b + (id2 >> 2)) * 8192 + hg * 2048 + (id2 & 3) * 128;
            asm volatile("global_load_dword %0, %1, off" : "=&v"(pf0) : "v"(p0) : "memory");
        }
        LOAD_X();
        MARK();
        const int lane = lane_id();
        const int aw = wid, g = aw >> 1, half = aw & 1;
        const int r = lane & 31, h = lane >> 5;
        const char* kb = (br ? kB : kA) + (size_t)(b * 16 + 4 * hg) * 131072;
        const char* vb = (br ? vB : vA) + (size_t)(b * 16 + 4 * hg) * 131072;
        unsigned doff = (unsigned)((8 * aw + ((lane >> 2) & 3)) * 256 + ((lane >> 4) * 4 + ((lane & 3) ^ ((2 * aw) & 3))) * 16);
        unsigned doff1 = doff ^ 16;
        const unsigned ddst = lds0 + LDS_KV + aw * 2048;
        const u32x4 rK = mkrsrc(kb, 524288), rV = mkrsrc(vb - 24576, 524288 + 24576);
#define KVDMA(rs) asm volatile("buffer_load_dwordx4 %0, %2, 0 offen nt lds\n\tbuffer_load_dwordx4 %1, %2, 0 offen offset:1024 nt lds" ::"v"(doff), "v"(doff1), "s"(rs) : "memory")
#define ADV() do { doff += 8192; doff1 += 8192; PIN(doff); PIN(doff1); } while (0)
#define NOP0() asm volatile("s_nop 0")
        WAIT_BAR(0);
        SETM0_ADD(ddst, 0); NOP0(); KVDMA(rK); ADV();
        SETM0_ADD(ddst, 8192); NOP0(); KVDMA(rK); ADV();
        SETM0_ADD(ddst, 16384); NOP0(); KVDMA(rK); ADV();
        SETM0_ADD(ddst, 32768); NOP0(); KVDMA(rV);
        SETM0_ADD(ddst, 24576); NOP0(); KVDMA(rK); ADV();
        asm volatile("" ::"v"(pf0));
        int kb0 = LDS_KV + 2048 * (r >> 3) + 1024 * ((r >> 2) & 1) + 64 * (r & 3) + 16 * (h ^ ((r >> 2) & 3)), kb1 = kb0 ^ 32; PIN(kb0); PIN(kb1);
        const int qq = (lane & 15) >> 2, pp = lane & 3, gam = (lane >> 4) & 1;
        const int vbase = LDS_KV + 32768 + 1024 * h + 64 * qq + 16 * ((2 * gam + (pp >> 1)) ^ h) + 8 * (pp & 1);
        const int qoff = LDS_QO + (16 * g + 2 * half) * 1024 + lane * 16;
        const int ooff = LDS_QO + (16 * g + 2 * half + (r >> 4)) * 1024 + (r & 15) * 16 + 8 * h;
        f32x16 o[4], sA, sB = f32x16{}, negm = f32x16{};
        bf16x8 qf[8], kf[8];
        f32x2 ls2 = f32x2{};
        float mref = 0.f;
        int vr = 16384, vw = 8192;
        int vp0 = vbase + vr, vp1 = vp0 ^ 32;
#pragma unroll
        for (int i = 0; i < 4; ++i) o[i] = f32x16{};
#define VRD(i) do { vl[i] = vtr(L + vp0 + ((i) >> 2) * 4096 + ((i) & 3) * 256); vh[i] = vtr(L + vp1 + ((i) >> 2) * 4096 + 2048 + ((i) & 3) * 256); } while (0)
#define KRD(s, slot) kf[s] = ldsr128(L + (((s) & 1) ? kb1 : kb0) + (slot) * 8192 + ((s) >> 1) * 256)
        WAIT_BAR(8);
#pragma unroll
        for (int s = 0; s < 8; ++s) KRD(s, 0);
        WAIT_BAR(6);
#define EXP2(X, i) do { float e0_ = __builtin_amdgcn_exp2f(X[i]), e1_ = __builtin_amdgcn_exp2f(X[(i) + 1]); PIN(e0_); PIN(e1_); X[i] = e0_; X[(i) + 1] = e1_; } while (0)
#define SUM2(X, i) ls2 += (f32x2){X[i], X[(i) + 1]}
#define QK(C, s) do { SBAR(); C = MFMA32(kf[s], qf[s], C); SBAR(); } while (0)
#define PV(i, p) do { SBAR(); o[(i) & 3] = MFMA32(cat8(vl[i], vh[i]), __builtin_bit_cast(bf16x8, p), o[(i) & 3]); SBAR(); } while (0)
#define XMAX(x) ({ auto rr_ = __builtin_amdgcn_permlane32_swap(__float_as_uint(x), __float_as_uint(x), false, false); \
                   __builtin_fmaxf(__uint_as_float(rr_[0]), __uint_as_float(rr_[1])); })
#define STEP(C, P, PH, BOUND) do { \
            s16x4 vl[8], vh[8]; \
            if (BOUND) { _Pragma("unroll") for (int s = 0; s < 8; ++s) qf[s] = ldsr128(L + qoff + (s >> 1) * 4096 + (s & 1) * 1024); C = f32x16{}; QK(C, 0); } \
            else { SBAR(); asm volatile("v_mfma_f32_32x32x16_bf16 %0, %2, %3, %1" : "=&v"(C), "+v"(negm) : "v"(kf[0]), "v"(qf[0])); SBAR(); } \
            VRD(0); VRD(1); EXP2(P, 8); \
            QK(C, 1); VRD(2); VRD(3); EXP2(P, 10); \
            QK(C, 2); VRD(4); VRD(5); EXP2(P, 12); \
            { const unsigned m_ = ddst + 32768 + vw; SETM0(m_); } \
            QK(C, 3); VRD(6); VRD(7); EXP2(P, 14); \
            vw = vr; vr += 8192; asm volatile("" : "+s"(vr)); \
            QK(C, 4); KVDMA(rV); \
            u32x4 p0 = {cvtpk(P[0], P[1]), cvtpk(P[2], P[3]), cvtpk(P[4], P[5]), cvtpk(P[6], P[7])}; PIN(p0); \
            vr = (vr == 24576) ? 0 : vr; asm volatile("" : "+s"(vr)); \
            QK(C, 5); SUM2(P, 0); SUM2(P, 2); SUM2(P, 4); SUM2(P, 6); PIN(ls2); \
            SETM0_ADD(ddst, (PH) * 8192); \
            QK(C, 6); KVDMA(rK); \
            u32x4 p1 = {cvtpk(P[8], P[9]), cvtpk(P[10], P[11]), cvtpk(P[12], P[13]), cvtpk(P[14], P[15])}; PIN(p1); \
            QK(C, 7); \
            SUM2(P, 8); SUM2(P, 10); SUM2(P, 12); SUM2(P, 14); PIN(ls2); \
            LGKM0(); \
            PV(0, p0); KRD(0, ((PH) + 1) & 3); KRD(1, ((PH) + 1) & 3); ADV(); vp0 = vbase + vr; vp1 = vp0 ^ 32; PIN(vp0); PIN(vp1); \
            PV(1, p0); KRD(2, ((PH) + 1) & 3); KRD(3, ((PH) + 1) & 3); \
            float a_ = MX3(C[0], C[1], C[2]), c_ = MX3(C[3], C[4], C[5]); \
            a_ = MX3(a_, C[6], C[7]); c_ = MX3(c_, C[8], C[9]); PIN(a_); PIN(c_); \
            PV(2, p0); KRD(4, ((PH) + 1) & 3); KRD(5, ((PH) + 1) & 3); \
            float d_ = MX3(C[10], C[11], C[12]), e_ = MX3(C[13], C[14], C[15]); \
            asm volatile("v_max3_f32 %0, %0, %1, %2\n\tv_max_f32 %0, %0, %3" : "+v"(a_) : "v"(c_), "v"(d_), "v"(e_)); \
            PV(3, p0); KRD(6, ((PH) + 1) & 3); KRD(7, ((PH) + 1) & 3); \
            float lprev = 0.f; \
            if (BOUND) { \
                lprev = ls2[0] + ls2[1]; ls2 = f32x2{}; \
                const float rm = XMAX(a_); mref = rm; \
                _Pragma("unroll") for (int i = 0; i < 16; ++i) { C[i] -= rm; negm[i] = -rm; } \
                PIN(negm); \
            } else if (__builtin_expect(__builtin_amdgcn_ballot_w64(a_ > THR) != 0, 0)) { \
                  \
                const float dl = __builtin_fmaxf(__builtin_ceilf(XMAX(a_)), 0.f); \
                const float al = __builtin_amdgcn_exp2f(-dl); mref += dl; ls2 *= al; \
                _Pragma("unroll") for (int i = 0; i < 16; ++i) { C[i] -= dl; negm[i] = -mref; } \
                PIN(negm); \
                _Pragma("unroll") for (int i = 0; i < 4; ++i) p1[i] = cvtpk(__uint_as_float(p1[i] << 16) * al, __uint_as_float(p1[i] & 0xffff0000u) * al); \
                _Pragma("unroll") for (int tt = 0; tt < 4; ++tt) \
                    _Pragma("unroll") for (int i = 0; i < 16; ++i) o[tt][i] *= al; \
                PIN(C); PIN(p1); PIN(ls2); PIN(o[0]); PIN(o[1]); PIN(o[2]); PIN(o[3]); \
            } \
            PV(4, p1); EXP2(C, 0); PV(5, p1); EXP2(C, 2); PV(6, p1); EXP2(C, 4); PV(7, p1); EXP2(C, 6); SBAR(); \
            if (BOUND) { \
                if (u > 0) { \
                    auto r2_ = __builtin_amdgcn_permlane32_swap(__float_as_uint(lprev), __float_as_uint(lprev), false, false); \
                    const float inv = 1.0f / (__uint_as_float(r2_[0]) + __uint_as_float(r2_[1])); \
                    _Pragma("unroll") for (int tt = 0; tt < 4; ++tt) \
                        _Pragma("unroll") for (int k = 0; k < 4; ++k) { \
                            u32x2 w = {cvtpk(o[tt][4 * k] * inv, o[tt][4 * k + 1] * inv), cvtpk(o[tt][4 * k + 2] * inv, o[tt][4 * k + 3] * inv)}; \
                            *(__attribute__((address_space(3))) u32x2*)(L + ooff + tt * 4096 + k * 256) = w; \
                        } \
                } \
                _Pragma("unroll") for (int i = 0; i < 4; ++i) o[i] = f32x16{}; \
            } \
            WAIT_BAR(6); \
        } while (0)
        for (int u = 0; u < 4; ++u) {
            STEP(sA, sB, 0, true); STEP(sB, sA, 1, false); STEP(sA, sB, 2, false); STEP(sB, sA, 3, false);
            for (int j = 0; j < 3; ++j) {
                STEP(sA, sB, 0, false); STEP(sB, sA, 1, false); STEP(sA, sB, 2, false); STEP(sB, sA, 3, false);
            }
        }
        { const int u = 4; STEP(sA, sB, 0, true); }
        WAITVM(0);
        PROF_END();
        __builtin_amdgcn_endpgm();
    } else {
        const int pw = wid - 4;
        unsigned wv0, wv1;
        const u32x4 rW = mkrsrc(wq + hg * 2048 + pw * 128, 576 * 8192);
        const unsigned wdst = lds0 + LDS_QO + pw * 8192;
        unsigned wp;
#define SET_W(u) do { const int l_ = lane_id(); wv0 = (unsigned)((l_ >> 3) * 8192 + (((l_ & 7) ^ (((l_ >> 4) & 1) << 2)) * 16)); wv1 = wv0 + (65536 - 1024); \
                      wp = (u) * 512; asm volatile("" : "+s"(wp)); } while (0)
#define ISSUE_W(i) do { SETM0_ADD(wdst, ((i) & 3) * 2048); asm volatile("s_nop 0"); BDMA2(wv0, wv1, rW, wp); wp += 131072; asm volatile("" : "+s"(wp)); } while (0)
#define START_W(u) do { SET_W(u); ISSUE_W(0); ISSUE_W(1); ISSUE_W(2); ISSUE_W(3); } while (0)
        const auto ro = __builtin_amdgcn_make_buffer_rsrc((void*)wo, 0, 4096 * 1152, 0x00020000);
        f32x4 acc[9][4];
        int so2, sp, sp2, so;
        START_W(0);
        LOAD_X();
        MARK();
        WAIT_BAR(0);
#define RD_A(k, buf) do { \
            af[buf][0] = cat8(vtr(L + wbase + ((k) & 3) * 2048), vtr(L + wbase + ((k) & 3) * 2048 + 512)); \
            af[buf][1] = cat8(vtr(L + wb1 + ((k) & 3) * 2048), vtr(L + wb1 + ((k) & 3) * 2048 + 512)); } while (0)
#define RD_B(k, buf) do { \
            if ((k) == 32) { xbase += 65536; PIN(xbase); } \
            bfr[buf][0] = ldsr128(L + xbase + (((k) >> 1) & 15) * 4096 + ((k) & 1) * 512); \
            bfr[buf][1] = ldsr128(L + xbase + (((k) >> 1) & 15) * 4096 + ((k) & 1) * 512 + 2048); } while (0)
#define QM(i, j, k) do { SBAR(); qa[i][j] = MFMA32(af[(k) & 1][i], bfr[(k) & 1][j], qa[i][j]); PIN(qa[i][j]); SBAR(); } while (0)
#define Q_PHASE(BARS) do { \
            const int lane = lane_id(), r = lane & 31, h = lane >> 5, qq = (lane & 15) >> 2, pp = lane & 3, gam = (lane >> 4) & 1; \
            int wbase = LDS_QO + pw * 8192 + 128 * (8 * h + qq) + 16 * (2 * gam + (pp & 1)) + 8 * (pp >> 1) + 64 * (qq >> 1); PIN(wbase); \
            int xbase = LDS_X + (r >> 4) * 1024 + h * 256 + (r & 15) * 16; PIN(xbase); \
            f32x16 qa[2][2]; \
            qa[0][0] = f32x16{}; qa[0][1] = f32x16{}; qa[1][0] = f32x16{}; qa[1][1] = f32x16{}; \
            bf16x8 af[2][2], bfr[2][2]; \
            int wb1 = wbase ^ 64; PIN(wb1); \
            WAITVM(6); \
            RD_A(0, 0); RD_B(0, 0); \
            _Pragma("unroll") for (int k = 0; k < 36; ++k) { \
                if (k + 4 < 36) SETM0_ADD(wdst, (k & 3) * 2048); \
                LGKM0(); \
                QM(0, 0, k); \
                if (k + 4 < 36) { BDMA2(wv0, wv1, rW, wp); wp += 131072; asm volatile("" : "+s"(wp)); } \
                if (k < 35) RD_B(k + 1, (k + 1) & 1); \
                if (BARS) PACE(QP - 24); \
                QM(0, 1, k); \
                if (k < 35) { \
                    if (k < 32) WAITVM(6); else if (k == 32) WAITVM(4); else if (k == 33) WAITVM(2); else WAITVM(0); \
                    RD_A(k + 1, (k + 1) & 1); \
                } \
                if (BARS) PACE(QP - 20); \
                QM(1, 0, k); \
                if (BARS) PACE(QP); \
                QM(1, 1, k); \
                if (BARS) PACE(QP - 8); \
                if (!(BARS) && k == 30) BAR(); \
                if (BARS && k <= 30 && k % 5 == 0) BAR(); \
            } \
            const int qwbase = LDS_QO + pw * 8192 + lane_id() * 16; \
            _Pragma("unroll") for (int t = 0; t < 2; ++t) \
                _Pragma("unroll") for (int m = 0; m < 2; ++m) \
                    _Pragma("unroll") for (int k = 0; k < 2; ++k) { \
                        const f32x16& a = qa[t][m]; \
                        u32x4 w = {cvtpk(a[8 * k] * QSCALE, a[8 * k + 1] * QSCALE), cvtpk(a[8 * k + 2] * QSCALE, a[8 * k + 3] * QSCALE), \
                                   cvtpk(a[8 * k + 4] * QSCALE, a[8 * k + 5] * QSCALE), cvtpk(a[8 * k + 6] * QSCALE, a[8 * k + 7] * QSCALE)}; \
                        *(__attribute__((address_space(3))) u32x4*)(L + qwbase + t * 4096 + m * 2048 + k * 1024) = w; \
                    } \
            SBAR(); \
        } while (0)
#define LOADJ(set, j) do { if ((j) < 8) raw[set][(j) & 7] = __builtin_amdgcn_raw_buffer_load_b128(ro, voa + ((j) & 3) * 1152, ((j) & 4) ? so2 : so, 0); \
                           else rc[(j) & 3] = __builtin_amdgcn_raw_buffer_load_b32(ro, voc + ((j) & 1) * 2304, ((j) & 2) ? sp2 : sp, 0); } while (0)
#define NEXT_ROWS() do { sp = so; sp2 = so2; so += 32 * 1152; asm volatile("" : "+s"(so)); so2 = so + 4608; asm volatile("" : "+s"(so2)); } while (0)
#define PERM1(dst, set, t, j) do { \
            if ((t) < 8) dst[j] = __builtin_amdgcn_perm(raw[set][2 * (j) + 1][((t) >> 1) & 3], raw[set][2 * (j)][((t) >> 1) & 3], ((t) & 1) ? 0x07060302u : 0x05040100u); \
            else dst[j] = __builtin_amdgcn_perm((unsigned)__builtin_amdgcn_mov_dpp((int)rc[j], 0xB1, 0xf, 0xf, true), rc[j], selc); } while (0)
#define O_PHASE(BARS, NEXT) do { \
            const int lane = lane_id(), c = lane & 15, q = lane >> 4; \
            int obase = LDS_QO + lane * 16; PIN(obase); \
            const int voa = (1024 * hg + 8 * q) * 1152 + (128 * pw + 8 * c) * 2; \
            const int voc = (1024 * hg + 8 * q + (c & 1)) * 1152 + (512 + 16 * pw + (c & 14)) * 2; \
            const unsigned selc = (c & 1) ? 0x03020706u : 0x05040100u; \
            bf16x8 of[4]; u32x4 ft[2], raw[2][8]; unsigned rc[4]; \
            _Pragma("unroll") for (int j = 0; j < 8; ++j) LOADJ(0, j); \
            NEXT_ROWS(); \
            _Pragma("unroll") for (int j = 0; j < 12; ++j) LOADJ(1, j); \
            NEXT_ROWS(); \
            SBAR(); \
            if (!(BARS)) { for (int j = 0; j < 8; ++j) BAR(); } \
            BAR(); \
            _Pragma("unroll") for (int m = 0; m < 4; ++m) of[m] = ldsr128(L + obase + m * 1024); \
            _Pragma("unroll") for (int j = 0; j < 4; ++j) PERM1(ft[0], 0, 0, j); \
            PIN(ft[0]); asm volatile("s_nop 1"); \
            _Pragma("unroll") for (int s = 0; s < 8; ++s) { \
                if (s == 6 && BARS) { SET_W(NEXT); if ((NEXT) >= 0) { ISSUE_W(0); ISSUE_W(1); } } \
                if (s == 7 && (NEXT) >= 0) { ISSUE_W(2); ISSUE_W(3); } \
                _Pragma("unroll") for (int t = 0; t < 9; ++t) \
                    _Pragma("unroll") for (int m = 0; m < 4; ++m) { \
                        const int g = s * 9 + t, nset = (t < 8) ? (s & 1) : ((s + 1) & 1), nt = (t < 8) ? t + 1 : 0, pos = t * 4 + m - 26; \
                        SBAR(); \
                        MFMA16_ACC(acc[t][m], ft[g & 1], of[m]); \
                        SBAR(); \
                        if (PPOS == 0 && m < 2 && g < 71) { PERM1(ft[(g + 1) & 1], nset, nt, 2 * m); PERM1(ft[(g + 1) & 1], nset, nt, 2 * m + 1); PIN(ft[(g + 1) & 1]); } \
                        if (PPOS == 1 && m == 1 && g < 71) { PERM1(ft[(g + 1) & 1], nset, nt, 0); PERM1(ft[(g + 1) & 1], nset, nt, 1); PERM1(ft[(g + 1) & 1], nset, nt, 2); PERM1(ft[(g + 1) & 1], nset, nt, 3); PIN(ft[(g + 1) & 1]); } \
                        if (s < 6 && pos >= 0 && pos % LSTEP == 0) LOADJ(s & 1, pos / LSTEP); \
                        if (s < 7 && pos >= 4 && (pos - 4) % LSTEP == 0 && (pos - 4) / LSTEP < 4) LOADJ(0, 8 + (pos - 4) / LSTEP); \
                        if (s > 0 && s < 7 && (pos + 36) % LSTEP == 0 && (pos + 36) / LSTEP < 8) LOADJ((s - 1) & 1, (pos + 36) / LSTEP); \
                        if (s > 0 && (pos + 32) % LSTEP == 0 && (pos + 32) / LSTEP < 4) LOADJ(0, 8 + (pos + 32) / LSTEP); \
                        if (s > 0 && s < 7 && pos == -4) NEXT_ROWS(); \
                        if (t == 8 && s < 7) of[m] = ldsr128(L + obase + (s + 1) * 4096 + m * 1024); \
                    } \
                SBAR(); \
                if (BARS && s < 7) LGKM_BAR(); \
                if (!(BARS)) MARK(); \
            } \
        } while (0)
        Q_PHASE(false);
        LGKM_BAR();
        BAR();
        {
            const int idx = pw * 64 + lane_id();
            const char* q0 = wq + (size_t)(18 * b) * 8192 + hg * 2048 + 512;
            const char* o0 = wo + (size_t)(1024 * hg + 32 * b) * 1152;
            const int i12 = idx / 12;
            const char* p0 = idx < 216 ? q0 + i12 * 8192 + (idx - 12 * i12) * 128 : o0 + (idx & 31) * 128;
            const char* p1 = o0 + 4096 + idx * 128;
            unsigned pf1, pf2;
            asm volatile("global_load_dword %0, %2, off\n\tglobal_load_dword %1, %3, off" : "=&v"(pf1), "=&v"(pf2) : "v"(p0), "v"(p1) : "memory");
            for (int j = 0; j < 6; ++j) BAR();
            WAITVM(0);
            asm volatile("" ::"v"(pf1), "v"(pf2));
        }
        START_W(1);
        BAR();
#pragma unroll
        for (int t = 0; t < 9; ++t)
#pragma unroll
            for (int m = 0; m < 4; ++m) acc[t][m] = f32x4{};
        so = 0; so2 = 4608; sp = 0; sp2 = 0;
        for (int i = 1; i < 4; ++i) {
            Q_PHASE(true);
            LGKM_BAR();
            O_PHASE(true, (i < 3 ? i + 1 : -1));
        }
        O_PHASE(false, -1);
        asm volatile("s_nop 15\n\ts_nop 15");
        f32x4* pp4 = (f32x4*)part + ((size_t)(wg * 4 + pw) * 36) * 64 + lane_id();
#pragma unroll
        for (int t = 0; t < 9; ++t)
#pragma unroll
            for (int m = 0; m < 4; ++m) pp4[(t * 4 + m) * 64] = acc[t][m];
        PROF_END();
        __builtin_amdgcn_endpgm();
    }
}

__global__ __launch_bounds__(256) void reduce_out(const float* __restrict__ part, unsigned short* __restrict__ out) {
    const int tid = threadIdx.x, lane = tid & 63, m = tid >> 6;
    const int blk = blockIdx.x, pw = blk & 3, br = (blk >> 2) & 1, b = blk >> 3;
    const int c = lane & 15, q = lane >> 4;
    f32x4 s[9];
#pragma unroll
    for (int t = 0; t < 9; ++t) s[t] = f32x4{};
#pragma unroll
    for (int hg = 0; hg < 4; ++hg) {
        const int wg = b * 8 + br * 4 + hg;
        const f32x4* p = (const f32x4*)part + ((size_t)(wg * 4 + pw) * 36) * 64 + lane;
#pragma unroll
        for (int t = 0; t < 9; ++t) s[t] += p[(t * 4 + m) * 64];
    }
    unsigned short* orow = out + ((size_t)(b * 64 + 16 * m + c) * 1152 + br * 576);
#pragma unroll
    for (int r = 0; r < 4; ++r) {
        u32x4 w = {cvtpk(s[0][r], s[1][r]), cvtpk(s[2][r], s[3][r]), cvtpk(s[4][r], s[5][r]), cvtpk(s[6][r], s[7][r])};
        *(u32x4*)(orow + 128 * pw + 32 * q + 8 * r) = w;
    }
    u32x2 w = {cvtpk(s[8][0], s[8][1]), cvtpk(s[8][2], s[8][3])};
    *(u32x2*)(orow + 512 + 16 * pw + 4 * q) = w;
}

extern "C" void kernel_launch(void* const* d_in, const int* in_sizes, int n_in, void* d_out, int out_size, void* d_ws, size_t ws_size,
                              hipStream_t stream) {
    (void)in_sizes; (void)n_in; (void)out_size; (void)ws_size;
    float* part = (float*)d_ws;
    fused_gqa<<<256, 512, 0, stream>>>((const char*)d_in[0], (const char*)d_in[1], (const char*)d_in[2], (const char*)d_in[3], (const char*)d_in[4],
                                       (const char*)d_in[5], (const char*)d_in[6], (const char*)d_in[7], (const char*)d_in[8], (const char*)d_in[9], part);
    reduce_out<<<256, 256, 0, stream>>>(part, (unsigned short*)d_out);
}
```

```cpp
#include <hip/hip_runtime.h>
#include <hip/hip_bf16.h>
#include <stdint.h>

typedef short bf16x8 __attribute__((ext_vector_type(8)));
typedef short s16x4 __attribute__((ext_vector_type(4)));
typedef float f32x16 __attribute__((ext_vector_type(16)));
typedef float f32x4 __attribute__((ext_vector_type(4)));
typedef float f32x2 __attribute__((ext_vector_type(2)));
typedef unsigned u32x4 __attribute__((ext_vector_type(4)));
typedef unsigned u32x2 __attribute__((ext_vector_type(2)));
typedef __attribute__((address_space(3))) char* lds_ptr;
typedef __attribute__((address_space(3))) const char* lds_cptr;

constexpr int LDS_X = 0, LDS_QO = 73728, LDS_KV = 106496, LDS_TOTAL = 163840;
constexpr float QSCALE = 0.08838834764831845f * 1.4426950408889634f;
constexpr float THR = 8.0f;
constexpr int XCH_BYTES = 256 * 4 * 3 * 4608, FLAG_OFF = 16 << 20, FLAG_BYTES = 256 * 4 * 128;
typedef __attribute__((address_space(1))) unsigned gu32;
#define LSTEP 2
#define QP 0
#define PPOS 0
#define PACE(n) do { if ((n) > 0) asm volatile("s_nop %0" ::"n"((n) > 16 ? 15 : (n) - 1)); if ((n) > 16) asm volatile("s_nop %0" ::"n"((n) > 32 ? 15 : (n) - 17)); if ((n) > 32) asm volatile("s_nop %0" ::"n"((n) - 33)); } while (0)

#define SBAR() __builtin_amdgcn_sched_barrier(0)
#define PIN(x) asm volatile("" : "+v"(x))
#define MFMA32(a, b, c) __builtin_amdgcn_mfma_f32_32x32x16_bf16(a, b, c, 0, 0, 0)
#define MFMA16(a, b, c) __builtin_amdgcn_mfma_f32_16x16x32_bf16(a, b, c, 0, 0, 0)
#define MFMA16_ACC(c, a, b) asm volatile("v_mfma_f32_16x16x32_bf16 %0, %1, %2, %0" : "+v"(c) : "v"(a), "v"(b))
#define PRE()
#define POST()
#define PROF_DECL
#define PROF_END()
#define WAITC(vm, lg) __builtin_amdgcn_s_waitcnt(((vm) & 15) | 0x70 | ((lg) << 8) | (((vm) >> 4) << 14))
#define WAIT_BAR(N) do { PRE(); asm volatile("" ::: "memory"); WAITC(N, 0); asm volatile("s_barrier" ::: "memory"); POST(); } while (0)
#define LGKM_BAR() WAIT_BAR(63)
#define LGKM0() do { asm volatile("" ::: "memory"); WAITC(63, 0); asm volatile("" ::: "memory"); } while (0)
#define BAR() do { PRE(); asm volatile("s_barrier" ::: "memory"); POST(); } while (0)
#define MARK() do { PRE(); POST(); } while (0)
#define WAITVM(N) asm volatile("s_waitcnt vmcnt(" #N ")" ::: "memory")
#define MX3(a, b, c) __builtin_fmaxf(__builtin_fmaxf((a), (b)), (c))

__device__ __forceinline__ unsigned cvtpk(float lo, float hi) {
    typedef float f32x2_t __attribute__((ext_vector_type(2)));
    typedef __bf16 bf16x2_t __attribute__((ext_vector_type(2)));
    return __builtin_bit_cast(unsigned, __builtin_convertvector((f32x2_t){lo, hi}, bf16x2_t));
}
__device__ __forceinline__ void dma1(unsigned voff, const char* sbase, unsigned lds_base) {
    unsigned sv;
    asm volatile("s_mov_b32 %0, m0\n\ts_mov_b32 m0, %3\n\ts_nop 0\n\tglobal_load_lds_dwordx4 %1, %2\n\ts_mov_b32 m0, %0"
                 : "=&s"(sv) : "v"(voff), "s"(sbase), "s"(lds_base) : "memory");
}
#define SETM0(v) asm volatile("s_mov_b32 m0, %0" ::"s"(v) : "memory")
#define SETM0_ADD(v, imm) asm volatile("s_add_u32 m0, %0, %1" ::"s"(v), "n"(imm) : "memory", "scc")
#define BDMA2NT(voff, rs) asm volatile("buffer_load_dwordx4 %0, %1, 0 offen nt lds\n\tbuffer_load_dwordx4 %0, %1, 0 offen offset:1024 nt lds" ::"v"(voff), "s"(rs) : "memory")
#define BDMA2(v0, v1, rs, so) asm volatile("buffer_load_dwordx4 %0, %2, %3 offen lds\n\tbuffer_load_dwordx4 %1, %2, %3 offen offset:1024 lds" ::"v"(v0), "v"(v1), "s"(rs), "s"(so) : "memory")
__device__ __forceinline__ u32x4 mkrsrc(const char* p, unsigned n) {
    const unsigned long long a = (unsigned long long)(uintptr_t)p;
    return (u32x4){(unsigned)a, (unsigned)(a >> 32) & 0xffffu, n, 0x00020000u};
}
__device__ __forceinline__ int lane_id() { int l; asm volatile("v_mbcnt_lo_u32_b32 %0, -1, 0\n\tv_mbcnt_hi_u32_b32 %0, -1, %0" : "=v"(l)); return l; }
__device__ __forceinline__ bf16x8 ldsr128(lds_cptr p) { return *(const __attribute__((address_space(3))) bf16x8*)p; }
__device__ __forceinline__ s16x4 vtr(lds_cptr p) {
    return __builtin_bit_cast(s16x4, __builtin_amdgcn_ds_read_tr16_b64_v4i16((__attribute__((address_space(3))) s16x4*)p));
}
__device__ __forceinline__ bf16x8 cat8(s16x4 a, s16x4 b) { return (bf16x8){a[0], a[1], a[2], a[3], b[0], b[1], b[2], b[3]}; }

__global__ __launch_bounds__(512, 2) void fused_gqa(const char* __restrict__ xA, const char* __restrict__ kA, const char* __restrict__ vA,
                                                    const char* __restrict__ wqA, const char* __restrict__ woA,
                                                    const char* __restrict__ xB, const char* __restrict__ kB, const char* __restrict__ vB,
                                                    const char* __restrict__ wqB, const char* __restrict__ woB, char* __restrict__ part,
                                                    unsigned short* __restrict__ out) {
    __shared__ __attribute__((aligned(1024))) char lds_raw[LDS_TOTAL];
    const int wid = __builtin_amdgcn_readfirstlane(threadIdx.x >> 6);
    const int wg = blockIdx.x, xg = wg & 7, br = xg >> 2, hg = xg & 3, b = wg >> 3;
    const unsigned lds0 = (unsigned)(uintptr_t)lds_raw;
    const lds_ptr L = (lds_ptr)lds_raw;
    PROF_DECL
    PRE(); POST();
    const char* xp = br ? xB : xA;
    const char* wq = br ? wqB : wqA;
    const char* wo = br ? woB : woA;

#define LOAD_X() do { \
        const int lane_ = lane_id(); \
        const unsigned voff = (unsigned)((lane_ & 15) * 1152 + (lane_ >> 4) * 16); \
        const char* xb = xp + (size_t)b * 64 * 1152; \
        _Pragma("unroll") for (int i = 0; i < 9; ++i) { \
            const int blk = wid * 9 + i, s = blk >> 2, m = blk & 3; \
            dma1(voff, xb + (m ^ hg) * 16 * 1152 + s * 64, lds0 + LDS_X + blk * 1024); \
        } } while (0)
    if (wid < 4) {
        unsigned pf0;
        {
            const int idx = wid * 18 + (lane_id() & 31);
            const int id2 = idx > 71 ? 71 : idx;
            const char* p0 = wq + (size_t)(18 * b + (id2 >> 2)) * 8192 + hg * 2048 + (id2 & 3) * 128;
            asm volatile("global_load_dword %0, %1, off" : "=&v"(pf0) : "v"(p0) : "memory");
        }
        LOAD_X();
        MARK();
        const int lane = lane_id();
        const int aw = wid, g = aw >> 1, half = aw & 1;
        const int r = lane & 31, h = lane >> 5;
        const char* kb = (br ? kB : kA) + (size_t)(b * 16 + 4 * hg) * 131072;
        const char* vb = (br ? vB : vA) + (size_t)(b * 16 + 4 * hg) * 131072;
        unsigned doff = (unsigned)((8 * aw + ((lane >> 2) & 3)) * 256 + ((lane >> 4) * 4 + ((lane & 3) ^ ((2 * aw) & 3))) * 16);
        unsigned doff1 = doff ^ 16;
        const unsigned ddst = lds0 + LDS_KV + aw * 2048;
        const u32x4 rK = mkrsrc(kb, 524288), rV = mkrsrc(vb - 24576, 524288 + 24576);
#define KVDMA(rs) asm volatile("buffer_load_dwordx4 %0, %2, 0 offen nt lds\n\tbuffer_load_dwordx4 %1, %2, 0 offen offset:1024 nt lds" ::"v"(doff), "v"(doff1), "s"(rs) : "memory")
#define ADV() do { doff += 8192; doff1 += 8192; PIN(doff); PIN(doff1); } while (0)
#define NOP0() asm volatile("s_nop 0")
        WAIT_BAR(0);
        SETM0_ADD(ddst, 0); NOP0(); KVDMA(rK); ADV();
        SETM0_ADD(ddst, 8192); NOP0(); KVDMA(rK); ADV();
        SETM0_ADD(ddst, 16384); NOP0(); KVDMA(rK); ADV();
        SETM0_ADD(ddst, 32768); NOP0(); KVDMA(rV);
        SETM0_ADD(ddst, 24576); NOP0(); KVDMA(rK); ADV();
        asm volatile("" ::"v"(pf0));
        int kb0 = LDS_KV + 2048 * (r >> 3) + 1024 * ((r >> 2) & 1) + 64 * (r & 3) + 16 * (h ^ ((r >> 2) & 3)), kb1 = kb0 ^ 32; PIN(kb0); PIN(kb1);
        const int qq = (lane & 15) >> 2, pp = lane & 3, gam = (lane >> 4) & 1;
        const int vbase = LDS_KV + 32768 + 1024 * h + 64 * qq + 16 * ((2 * gam + (pp >> 1)) ^ h) + 8 * (pp & 1);
        const int qoff = LDS_QO + (16 * g + 2 * half) * 1024 + lane * 16;
        const int ooff = LDS_QO + (16 * g + 2 * half + (r >> 4)) * 1024 + (r & 15) * 16 + 8 * h;
        f32x16 o[4], sA, sB = f32x16{}, negm = f32x16{};
        bf16x8 qf[8], kf[8];
        f32x2 ls2 = f32x2{};
        float mref = 0.f;
        int vr = 16384, vw = 8192;
        int vp0 = vbase + vr, vp1 = vp0 ^ 32;
#pragma unroll
        for (int i = 0; i < 4; ++i) o[i] = f32x16{};
#define VRD(i) do { vl[i] = vtr(L + vp0 + ((i) >> 2) * 4096 + ((i) & 3) * 256); vh[i] = vtr(L + vp1 + ((i) >> 2) * 4096 + 2048 + ((i) & 3) * 256); } while (0)
#define KRD(s, slot) kf[s] = ldsr128(L + (((s) & 1) ? kb1 : kb0) + (slot) * 8192 + ((s) >> 1) * 256)
        WAIT_BAR(8);
#pragma unroll
        for (int s = 0; s < 8; ++s) KRD(s, 0);
        WAIT_BAR(6);
#define EXP2(X, i) do { float e0_ = __builtin_amdgcn_exp2f(X[i]), e1_ = __builtin_amdgcn_exp2f(X[(i) + 1]); PIN(e0_); PIN(e1_); X[i] = e0_; X[(i) + 1] = e1_; } while (0)
#define SUM2(X, i) ls2 += (f32x2){X[i], X[(i) + 1]}
#define QK(C, s) do { SBAR(); C = MFMA32(kf[s], qf[s], C); SBAR(); } while (0)
#define PV(i, p) do { SBAR(); o[(i) & 3] = MFMA32(cat8(vl[i], vh[i]), __builtin_bit_cast(bf16x8, p), o[(i) & 3]); SBAR(); } while (0)
#define XMAX(x) ({ auto rr_ = __builtin_amdgcn_permlane32_swap(__float_as_uint(x), __float_as_uint(x), false, false); \
                   __builtin_fmaxf(__uint_as_float(rr_[0]), __uint_as_float(rr_[1])); })
#define STEP(C, P, PH, BOUND) do { \
            s16x4 vl[8], vh[8]; \
            if (BOUND) { _Pragma("unroll") for (int s = 0; s < 8; ++s) qf[s] = ldsr128(L + qoff + (s >> 1) * 4096 + (s & 1) * 1024); C = f32x16{}; QK(C, 0); } \
            else { SBAR(); asm volatile("v_mfma_f32_32x32x16_bf16 %0, %2, %3, %1" : "=&v"(C), "+v"(negm) : "v"(kf[0]), "v"(qf[0])); SBAR(); } \
            VRD(0); VRD(1); EXP2(P, 8); \
            QK(C, 1); VRD(2); VRD(3); EXP2(P, 10); \
            QK(C, 2); VRD(4); VRD(5); EXP2(P, 12); \
            { const unsigned m_ = ddst + 32768 + vw; SETM0(m_); } \
            QK(C, 3); VRD(6); VRD(7); EXP2(P, 14); \
            vw = vr; vr += 8192; asm volatile("" : "+s"(vr)); \
            QK(C, 4); KVDMA(rV); \
            u32x4 p0 = {cvtpk(P[0], P[1]), cvtpk(P[2], P[3]), cvtpk(P[4], P[5]), cvtpk(P[6], P[7])}; PIN(p0); \
            vr = (vr == 24576) ? 0 : vr; asm volatile("" : "+s"(vr)); \
            QK(C, 5); SUM2(P, 0); SUM2(P, 2); SUM2(P, 4); SUM2(P, 6); PIN(ls2); \
            SETM0_ADD(ddst, (PH) * 8192); \
            QK(C, 6); KVDMA(rK); \
            u32x4 p1 = {cvtpk(P[8], P[9]), cvtpk(P[10], P[11]), cvtpk(P[12], P[13]), cvtpk(P[14], P[15])}; PIN(p1); \
            QK(C, 7); \
            SUM2(P, 8); SUM2(P, 10); SUM2(P, 12); SUM2(P, 14); PIN(ls2); \
            LGKM0(); \
            PV(0, p0); KRD(0, ((PH) + 1) & 3); KRD(1, ((PH) + 1) & 3); ADV(); vp0 = vbase + vr; vp1 = vp0 ^ 32; PIN(vp0); PIN(vp1); \
            PV(1, p0); KRD(2, ((PH) + 1) & 3); KRD(3, ((PH) + 1) & 3); \
            float a_ = MX3(C[0], C[1], C[2]), c_ = MX3(C[3], C[4], C[5]); \
            a_ = MX3(a_, C[6], C[7]); c_ = MX3(c_, C[8], C[9]); PIN(a_); PIN(c_); \
            PV(2, p0); KRD(4, ((PH) + 1) & 3); KRD(5, ((PH) + 1) & 3); \
            float d_ = MX3(C[10], C[11], C[12]), e_ = MX3(C[13], C[14], C[15]); \
            asm volatile("v_max3_f32 %0, %0, %1, %2\n\tv_max_f32 %0, %0, %3" : "+v"(a_) : "v"(c_), "v"(d_), "v"(e_)); \
            PV(3, p0); KRD(6, ((PH) + 1) & 3); KRD(7, ((PH) + 1) & 3); \
            float lprev = 0.f; \
            if (BOUND) { \
                lprev = ls2[0] + ls2[1]; ls2 = f32x2{}; \
                const float rm = XMAX(a_); mref = rm; \
                _Pragma("unroll") for (int i = 0; i < 16; ++i) { C[i] -= rm; negm[i] = -rm; } \
                PIN(negm); \
            } else if (__builtin_expect(__builtin_amdgcn_ballot_w64(a_ > THR) != 0, 0)) { \
                  \
                const float dl = __builtin_fmaxf(__builtin_ceilf(XMAX(a_)), 0.f); \
                const float al = __builtin_amdgcn_exp2f(-dl); mref += dl; ls2 *= al; \
                _Pragma("unroll") for (int i = 0; i < 16; ++i) { C[i] -= dl; negm[i] = -mref; } \
                PIN(negm); \
                _Pragma("unroll") for (int i = 0; i < 4; ++i) p1[i] = cvtpk(__uint_as_float(p1[i] << 16) * al, __uint_as_float(p1[i] & 0xffff0000u) * al); \
                _Pragma("unroll") for (int tt = 0; tt < 4; ++tt) \
                    _Pragma("unroll") for (int i = 0; i < 16; ++i) o[tt][i] *= al; \
                PIN(C); PIN(p1); PIN(ls2); PIN(o[0]); PIN(o[1]); PIN(o[2]); PIN(o[3]); \
            } \
            PV(4, p1); EXP2(C, 0); PV(5, p1); EXP2(C, 2); PV(6, p1); EXP2(C, 4); PV(7, p1); EXP2(C, 6); SBAR(); \
            if (BOUND) { \
                if (u > 0) { \
                    auto r2_ = __builtin_amdgcn_permlane32_swap(__float_as_uint(lprev), __float_as_uint(lprev), false, false); \
                    const float inv = 1.0f / (__uint_as_float(r2_[0]) + __uint_as_float(r2_[1])); \
                    _Pragma("unroll") for (int tt = 0; tt < 4; ++tt) \
                        _Pragma("unroll") for (int k = 0; k < 4; ++k) { \
                            u32x2 w = {cvtpk(o[tt][4 * k] * inv, o[tt][4 * k + 1] * inv), cvtpk(o[tt][4 * k + 2] * inv, o[tt][4 * k + 3] * inv)}; \
                            *(__attribute__((address_space(3))) u32x2*)(L + ooff + tt * 4096 + k * 256) = w; \
                        } \
                } \
                _Pragma("unroll") for (int i = 0; i < 4; ++i) o[i] = f32x16{}; \
            } \
            WAIT_BAR(6); \
        } while (0)
        for (int u = 0; u < 4; ++u) {
            STEP(sA, sB, 0, true); STEP(sB, sA, 1, false); STEP(sA, sB, 2, false); STEP(sB, sA, 3, false);
            for (int j = 0; j < 3; ++j) {
                STEP(sA, sB, 0, false); STEP(sB, sA, 1, false); STEP(sA, sB, 2, false); STEP(sB, sA, 3, false);
            }
        }
        { const int u = 4; STEP(sA, sB, 0, true); }
        WAITVM(0);
        PROF_END();
        __builtin_amdgcn_endpgm();
    } else {
        const int pw = wid - 4;
        unsigned wv0, wv1;
        const u32x4 rW = mkrsrc(wq + hg * 2048 + pw * 128, 576 * 8192);
        const unsigned wdst = lds0 + LDS_QO + pw * 8192;
        unsigned wp;
#define SET_W(u) do { const int l_ = lane_id(); wv0 = (unsigned)((l_ >> 3) * 8192 + (((l_ & 7) ^ (((l_ >> 4) & 1) << 2)) * 16)); wv1 = wv0 + (65536 - 1024); \
                      wp = (u) * 512; asm volatile("" : "+s"(wp)); } while (0)
#define ISSUE_W(i) do { SETM0_ADD(wdst, ((i) & 3) * 2048); asm volatile("s_nop 0"); BDMA2(wv0, wv1, rW, wp); wp += 131072; asm volatile("" : "+s"(wp)); } while (0)
#define START_W(u) do { SET_W(u); ISSUE_W(0); ISSUE_W(1); ISSUE_W(2); ISSUE_W(3); } while (0)
        const auto ro = __builtin_amdgcn_make_buffer_rsrc((void*)wo, 0, 4096 * 1152, 0x00020000);
        f32x4 acc[9][4];
        int so2, sp, sp2, so;
        START_W(0);
        LOAD_X();
        MARK();
        WAIT_BAR(0);
#define RD_A(k, buf) do { \
            af[buf][0] = cat8(vtr(L + wbase + ((k) & 3) * 2048), vtr(L + wbase + ((k) & 3) * 2048 + 512)); \
            af[buf][1] = cat8(vtr(L + wb1 + ((k) & 3) * 2048), vtr(L + wb1 + ((k) & 3) * 2048 + 512)); } while (0)
#define RD_B(k, buf) do { \
            if ((k) == 32) { xbase += 65536; PIN(xbase); } \
            bfr[buf][0] = ldsr128(L + xbase + (((k) >> 1) & 15) * 4096 + ((k) & 1) * 512); \
            bfr[buf][1] = ldsr128(L + xbase + (((k) >> 1) & 15) * 4096 + ((k) & 1) * 512 + 2048); } while (0)
#define QM(i, j, k) do { SBAR(); qa[i][j] = MFMA32(af[(k) & 1][i], bfr[(k) & 1][j], qa[i][j]); PIN(qa[i][j]); SBAR(); } while (0)
#define Q_PHASE(BARS) do { \
            const int lane = lane_id(), r = lane & 31, h = lane >> 5, qq = (lane & 15) >> 2, pp = lane & 3, gam = (lane >> 4) & 1; \
            int wbase = LDS_QO + pw * 8192 + 128 * (8 * h + qq) + 16 * (2 * gam + (pp & 1)) + 8 * (pp >> 1) + 64 * (qq >> 1); PIN(wbase); \
            int xbase = LDS_X + (r >> 4) * 1024 + h * 256 + (r & 15) * 16; PIN(xbase); \
            f32x16 qa[2][2]; \
            qa[0][0] = f32x16{}; qa[0][1] = f32x16{}; qa[1][0] = f32x16{}; qa[1][1] = f32x16{}; \
            bf16x8 af[2][2], bfr[2][2]; \
            int wb1 = wbase ^ 64; PIN(wb1); \
            WAITVM(6); \
            RD_A(0, 0); RD_B(0, 0); \
            _Pragma("unroll") for (int k = 0; k < 36; ++k) { \
                if (k + 4 < 36) SETM0_ADD(wdst, (k & 3) * 2048); \
                LGKM0(); \
                QM(0, 0, k); \
                if (k + 4 < 36) { BDMA2(wv0, wv1, rW, wp); wp += 131072; asm volatile("" : "+s"(wp)); } \
                if (k < 35) RD_B(k + 1, (k + 1) & 1); \
                if (BARS) PACE(QP - 24); \
                QM(0, 1, k); \
                if (k < 35) { \
                    if (k < 32) WAITVM(6); else if (k == 32) WAITVM(4); else if (k == 33) WAITVM(2); else WAITVM(0); \
                    RD_A(k + 1, (k + 1) & 1); \
                } \
                if (BARS) PACE(QP - 20); \
                QM(1, 0, k); \
                if (BARS) PACE(QP); \
                QM(1, 1, k); \
                if (BARS) PACE(QP - 8); \
                if (!(BARS) && k == 30) BAR(); \
                if (BARS && k <= 30 && k % 5 == 0) BAR(); \
            } \
            const int qwbase = LDS_QO + pw * 8192 + lane_id() * 16; \
            _Pragma("unroll") for (int t = 0; t < 2; ++t) \
                _Pragma("unroll") for (int m = 0; m < 2; ++m) \
                    _Pragma("unroll") for (int k = 0; k < 2; ++k) { \
                        const f32x16& a = qa[t][m]; \
                        u32x4 w = {cvtpk(a[8 * k] * QSCALE, a[8 * k + 1] * QSCALE), cvtpk(a[8 * k + 2] * QSCALE, a[8 * k + 3] * QSCALE), \
                                   cvtpk(a[8 * k + 4] * QSCALE, a[8 * k + 5] * QSCALE), cvtpk(a[8 * k + 6] * QSCALE, a[8 * k + 7] * QSCALE)}; \
                        *(__attribute__((address_space(3))) u32x4*)(L + qwbase + t * 4096 + m * 2048 + k * 1024) = w; \
                    } \
            SBAR(); \
        } while (0)
#define LOADJ(set, j) do { if ((j) < 8) raw[set][(j) & 7] = __builtin_amdgcn_raw_buffer_load_b128(ro, voa + ((j) & 3) * 1152, ((j) & 4) ? so2 : so, 0); \
                           else rc[(j) & 3] = __builtin_amdgcn_raw_buffer_load_b32(ro, voc + ((j) & 1) * 2304, ((j) & 2) ? sp2 : sp, 0); } while (0)
#define NEXT_ROWS() do { sp = so; sp2 = so2; so += 32 * 1152; asm volatile("" : "+s"(so)); so2 = so + 4608; asm volatile("" : "+s"(so2)); } while (0)
#define PERM1(dst, set, t, j) do { \
            if ((t) < 8) dst[j] = __builtin_amdgcn_perm(raw[set][2 * (j) + 1][((t) >> 1) & 3], raw[set][2 * (j)][((t) >> 1) & 3], ((t) & 1) ? 0x07060302u : 0x05040100u); \
            else dst[j] = __builtin_amdgcn_perm((unsigned)__builtin_amdgcn_mov_dpp((int)rc[j], 0xB1, 0xf, 0xf, true), rc[j], selc); } while (0)
#define O_PHASE(BARS, NEXT) do { \
            const int lane = lane_id(), c = lane & 15, q = lane >> 4; \
            int obase = LDS_QO + lane * 16; PIN(obase); \
            const int voa = (1024 * hg + 8 * q) * 1152 + (128 * pw + 8 * c) * 2; \
            const int voc = (1024 * hg + 8 * q + (c & 1)) * 1152 + (512 + 16 * pw + (c & 14)) * 2; \
            const unsigned selc = (c & 1) ? 0x03020706u : 0x05040100u; \
            bf16x8 of[4]; u32x4 ft[2], raw[2][8]; unsigned rc[4]; \
            _Pragma("unroll") for (int j = 0; j < 8; ++j) LOADJ(0, j); \
            NEXT_ROWS(); \
            _Pragma("unroll") for (int j = 0; j < 12; ++j) LOADJ(1, j); \
            NEXT_ROWS(); \
            SBAR(); \
            if (!(BARS)) { for (int j = 0; j < 8; ++j) BAR(); } \
            BAR(); \
            _Pragma("unroll") for (int m = 0; m < 4; ++m) of[m] = ldsr128(L + obase + m * 1024); \
            _Pragma("unroll") for (int j = 0; j < 4; ++j) PERM1(ft[0], 0, 0, j); \
            PIN(ft[0]); asm volatile("s_nop 1"); \
            _Pragma("unroll") for (int s = 0; s < 8; ++s) { \
                if (s == 6 && BARS) { SET_W(NEXT); if ((NEXT) >= 0) { ISSUE_W(0); ISSUE_W(1); } } \
                if (s == 7 && (NEXT) >= 0) { ISSUE_W(2); ISSUE_W(3); } \
                _Pragma("unroll") for (int t = 0; t < 9; ++t) \
                    _Pragma("unroll") for (int m = 0; m < 4; ++m) { \
                        const int g = s * 9 + t, nset = (t < 8) ? (s & 1) : ((s + 1) & 1), nt = (t < 8) ? t + 1 : 0, pos = t * 4 + m - 26; \
                        SBAR(); \
                        MFMA16_ACC(acc[t][m], ft[g & 1], of[m]); \
                        SBAR(); \
                        if (PPOS == 0 && m < 2 && g < 71) { PERM1(ft[(g + 1) & 1], nset, nt, 2 * m); PERM1(ft[(g + 1) & 1], nset, nt, 2 * m + 1); PIN(ft[(g + 1) & 1]); } \
                        if (PPOS == 1 && m == 1 && g < 71) { PERM1(ft[(g + 1) & 1], nset, nt, 0); PERM1(ft[(g + 1) & 1], nset, nt, 1); PERM1(ft[(g + 1) & 1], nset, nt, 2); PERM1(ft[(g + 1) & 1], nset, nt, 3); PIN(ft[(g + 1) & 1]); } \
                        if (s < 6 && pos >= 0 && pos % LSTEP == 0) LOADJ(s & 1, pos / LSTEP); \
                        if (s < 7 && pos >= 4 && (pos - 4) % LSTEP == 0 && (pos - 4) / LSTEP < 4) LOADJ(0, 8 + (pos - 4) / LSTEP); \
                        if (s > 0 && s < 7 && (pos + 36) % LSTEP == 0 && (pos + 36) / LSTEP < 8) LOADJ((s - 1) & 1, (pos + 36) / LSTEP); \
                        if (s > 0 && (pos + 32) % LSTEP == 0 && (pos + 32) / LSTEP < 4) LOADJ(0, 8 + (pos + 32) / LSTEP); \
                        if (s > 0 && s < 7 && pos == -4) NEXT_ROWS(); \
                        if (t == 8 && s < 7) of[m] = ldsr128(L + obase + (s + 1) * 4096 + m * 1024); \
                    } \
                SBAR(); \
                if (BARS && s < 7) LGKM_BAR(); \
                if (!(BARS)) MARK(); \
            } \
        } while (0)
        Q_PHASE(false);
        LGKM_BAR();
        BAR();
        {
            const int idx = pw * 64 + lane_id();
            const char* q0 = wq + (size_t)(18 * b) * 8192 + hg * 2048 + 512;
            const char* o0 = wo + (size_t)(1024 * hg + 32 * b) * 1152;
            const int i12 = idx / 12;
            const char* p0 = idx < 216 ? q0 + i12 * 8192 + (idx - 12 * i12) * 128 : o0 + (idx & 31) * 128;
            const char* p1 = o0 + 4096 + idx * 128;
            unsigned pf1, pf2;
            asm volatile("global_load_dword %0, %2, off\n\tglobal_load_dword %1, %3, off" : "=&v"(pf1), "=&v"(pf2) : "v"(p0), "v"(p1) : "memory");
            for (int j = 0; j < 6; ++j) BAR();
            WAITVM(0);
            asm volatile("" ::"v"(pf1), "v"(pf2));
        }
        START_W(1);
        BAR();
#pragma unroll
        for (int t = 0; t < 9; ++t)
#pragma unroll
            for (int m = 0; m < 4; ++m) acc[t][m] = f32x4{};
        so = 0; so2 = 4608; sp = 0; sp2 = 0;
        for (int i = 1; i < 4; ++i) {
            Q_PHASE(true);
            LGKM_BAR();
            O_PHASE(true, (i < 3 ? i + 1 : -1));
        }
        O_PHASE(false, -1);
        asm volatile("s_nop 15\n\ts_nop 15");
        const int lane = lane_id();
        const auto rx = __builtin_amdgcn_make_buffer_rsrc((void*)part, 0, XCH_BYTES, 0x00020000);
        gu32* flags = (gu32*)(part + FLAG_OFF);
#pragma unroll
        for (int j = 1; j < 4; ++j) {
            const int sb = (((wg ^ j) * 4 + pw) * 3 + j - 1) * 4608;
#pragma unroll
            for (int i = 0; i < 4; ++i) {
                const f32x4 &a0 = acc[2 * i][j], &a1 = acc[2 * i + 1][j];
                u32x4 w = {cvtpk(a0[0], a0[1]), cvtpk(a0[2], a0[3]), cvtpk(a1[0], a1[1]), cvtpk(a1[2], a1[3])};
                __builtin_amdgcn_raw_buffer_store_b128(w, rx, lane * 16 + i * 1024, sb, 16);
            }
            u32x2 w = {cvtpk(acc[8][j][0], acc[8][j][1]), cvtpk(acc[8][j][2], acc[8][j][3])};
            __builtin_amdgcn_raw_buffer_store_b64(w, rx, lane * 8 + 4096, sb, 16);
        }
        WAITVM(0);
        if (lane < 3) __hip_atomic_store(flags + ((wg ^ (lane + 1)) * 4 + pw) * 32 + lane, 1u, __ATOMIC_RELAXED, __HIP_MEMORY_SCOPE_AGENT);
        {
            gu32* mine = flags + (wg * 4 + pw) * 32 + (lane < 3 ? lane : 0);
            for (unsigned spins = 0; spins < (1u << 17); ++spins) {
                const unsigned v = __hip_atomic_load(mine, __ATOMIC_RELAXED, __HIP_MEMORY_SCOPE_AGENT);
                if (__builtin_amdgcn_ballot_w64(v != 1u) == 0) break;
                __builtin_amdgcn_s_sleep(1);
            }
            __builtin_amdgcn_fence(__ATOMIC_ACQUIRE, "agent");
        }
        u32x4 rv[3][4]; u32x2 rw[3];
#pragma unroll
        for (int j = 0; j < 3; ++j) {
            const int sb = ((wg * 4 + pw) * 3 + j) * 4608;
#pragma unroll
            for (int i = 0; i < 4; ++i) rv[j][i] = __builtin_amdgcn_raw_buffer_load_b128(rx, lane * 16 + i * 1024, sb, 0);
            rw[j] = __builtin_amdgcn_raw_buffer_load_b64(rx, lane * 8 + 4096, sb, 0);
        }
#define BLO(x) __uint_as_float((x) << 16)
#define BHI(x) __uint_as_float((x) & 0xffff0000u)
#pragma unroll
        for (int j = 0; j < 3; ++j) {
#pragma unroll
            for (int i = 0; i < 4; ++i) {
                acc[2 * i][0] += (f32x4){BLO(rv[j][i][0]), BHI(rv[j][i][0]), BLO(rv[j][i][1]), BHI(rv[j][i][1])};
                acc[2 * i + 1][0] += (f32x4){BLO(rv[j][i][2]), BHI(rv[j][i][2]), BLO(rv[j][i][3]), BHI(rv[j][i][3])};
            }
            acc[8][0] += (f32x4){BLO(rw[j][0]), BHI(rw[j][0]), BLO(rw[j][1]), BHI(rw[j][1])};
        }
        unsigned short* orow = out + ((size_t)(b * 64 + 16 * hg + (lane & 15)) * 1152 + br * 576);
        const int q = lane >> 4;
#pragma unroll
        for (int r = 0; r < 4; ++r) {
            u32x4 w = {cvtpk(acc[0][0][r], acc[1][0][r]), cvtpk(acc[2][0][r], acc[3][0][r]), cvtpk(acc[4][0][r], acc[5][0][r]), cvtpk(acc[6][0][r], acc[7][0][r])};
            *(u32x4*)(orow + 128 * pw + 32 * q + 8 * r) = w;
        }
        u32x2 w = {cvtpk(acc[8][0][0], acc[8][0][1]), cvtpk(acc[8][0][2], acc[8][0][3])};
        *(u32x2*)(orow + 512 + 16 * pw + 4 * q) = w;
        PROF_END();
        __builtin_amdgcn_endpgm();
    }
}

extern "C" void kernel_launch(void* const* d_in, const int* in_sizes, int n_in, void* d_out, int out_size, void* d_ws, size_t ws_size,
                              hipStream_t stream) {
    (void)in_sizes; (void)n_in; (void)out_size; (void)ws_size;
    char* part = (char*)d_ws;
    hipMemsetAsync(part + FLAG_OFF, 0, FLAG_BYTES, stream);
    fused_gqa<<<256, 512, 0, stream>>>((const char*)d_in[0], (const char*)d_in[1], (const char*)d_in[2], (const char*)d_in[3], (const char*)d_in[4],
                                       (const char*)d_in[5], (const char*)d_in[6], (const char*)d_in[7], (const char*)d_in[8], (const char*)d_in[9], part,
                                       (unsigned short*)d_out);
}
```

```cpp
#include <hip/hip_runtime.h>
#include <hip/hip_bf16.h>
#include <stdint.h>

typedef short bf16x8 __attribute__((ext_vector_type(8)));
typedef short s16x4 __attribute__((ext_vector_type(4)));
typedef float f32x16 __attribute__((ext_vector_type(16)));
typedef float f32x4 __attribute__((ext_vector_type(4)));
typedef float f32x2 __attribute__((ext_vector_type(2)));
typedef unsigned u32x4 __attribute__((ext_vector_type(4)));
typedef unsigned u32x2 __attribute__((ext_vector_type(2)));
typedef __attribute__((address_space(3))) char* lds_ptr;
typedef __attribute__((address_space(3))) const char* lds_cptr;

constexpr int LDS_X = 0, LDS_QO = 73728, LDS_KV = 106496, LDS_TOTAL = 163840;
constexpr float QSCALE = 0.08838834764831845f * 1.4426950408889634f;
constexpr float THR = 8.0f;
constexpr int XCH_BYTES = 256 * 4 * 3 * 4608, FLAG_OFF = 16 << 20, FLAG_BYTES = 64 * 256;
typedef __attribute__((address_space(1))) unsigned gu32;
#define LSTEP 2
#define QP 0
#define PPOS 0
#define PACE(n) do { if ((n) > 0) asm volatile("s_nop %0" ::"n"((n) > 16 ? 15 : (n) - 1)); if ((n) > 16) asm volatile("s_nop %0" ::"n"((n) > 32 ? 15 : (n) - 17)); if ((n) > 32) asm volatile("s_nop %0" ::"n"((n) - 33)); } while (0)

#define SBAR() __builtin_amdgcn_sched_barrier(0)
#define PIN(x) asm volatile("" : "+v"(x))
#define MFMA32(a, b, c) __builtin_amdgcn_mfma_f32_32x32x16_bf16(a, b, c, 0, 0, 0)
#define MFMA16(a, b, c) __builtin_amdgcn_mfma_f32_16x16x32_bf16(a, b, c, 0, 0, 0)
#define MFMA16_ACC(c, a, b) asm volatile("v_mfma_f32_16x16x32_bf16 %0, %1, %2, %0" : "+v"(c) : "v"(a), "v"(b))
#define PRE()
#define POST()
#define PROF_DECL
#define RT(i)
#define PROF_END()
#define WAITC(vm, lg) __builtin_amdgcn_s_waitcnt(((vm) & 15) | 0x70 | ((lg) << 8) | (((vm) >> 4) << 14))
#define WAIT_BAR(N) do { PRE(); asm volatile("" ::: "memory"); WAITC(N, 0); asm volatile("s_barrier" ::: "memory"); POST(); } while (0)
#define LGKM_BAR() WAIT_BAR(63)
#define LGKM0() do { asm volatile("" ::: "memory"); WAITC(63, 0); asm volatile("" ::: "memory"); } while (0)
#define BAR() do { PRE(); asm volatile("s_barrier" ::: "memory"); POST(); } while (0)
#define MARK() do { PRE(); POST(); } while (0)
#define WAITVM(N) asm volatile("s_waitcnt vmcnt(" #N ")" ::: "memory")
#define MX3(a, b, c) __builtin_fmaxf(__builtin_fmaxf((a), (b)), (c))

__device__ __forceinline__ unsigned cvtpk(float lo, float hi) {
    typedef float f32x2_t __attribute__((ext_vector_type(2)));
    typedef __bf16 bf16x2_t __attribute__((ext_vector_type(2)));
    return __builtin_bit_cast(unsigned, __builtin_convertvector((f32x2_t){lo, hi}, bf16x2_t));
}
__device__ __forceinline__ void dma1(unsigned voff, const char* sbase, unsigned lds_base) {
    unsigned sv;
    asm volatile("s_mov_b32 %0, m0\n\ts_mov_b32 m0, %3\n\ts_nop 0\n\tglobal_load_lds_dwordx4 %1, %2\n\ts_mov_b32 m0, %0"
                 : "=&s"(sv) : "v"(voff), "s"(sbase), "s"(lds_base) : "memory");
}
#define SETM0(v) asm volatile("s_mov_b32 m0, %0" ::"s"(v) : "memory")
#define SETM0_ADD(v, imm) asm volatile("s_add_u32 m0, %0, %1" ::"s"(v), "n"(imm) : "memory", "scc")
#define BDMA2NT(voff, rs) asm volatile("buffer_load_dwordx4 %0, %1, 0 offen nt lds\n\tbuffer_load_dwordx4 %0, %1, 0 offen offset:1024 nt lds" ::"v"(voff), "s"(rs) : "memory")
#define BDMA2(v0, v1, rs, so) asm volatile("buffer_load_dwordx4 %0, %2, %3 offen lds\n\tbuffer_load_dwordx4 %1, %2, %3 offen offset:1024 lds" ::"v"(v0), "v"(v1), "s"(rs), "s"(so) : "memory")
__device__ __forceinline__ u32x4 mkrsrc(const char* p, unsigned n) {
    const unsigned long long a = (unsigned long long)(uintptr_t)p;
    return (u32x4){(unsigned)a, (unsigned)(a >> 32) & 0xffffu, n, 0x00020000u};
}
__device__ __forceinline__ int lane_id() { int l; asm volatile("v_mbcnt_lo_u32_b32 %0, -1, 0\n\tv_mbcnt_hi_u32_b32 %0, -1, %0" : "=v"(l)); return l; }
__device__ __forceinline__ bf16x8 ldsr128(lds_cptr p) { return *(const __attribute__((address_space(3))) bf16x8*)p; }
__device__ __forceinline__ s16x4 vtr(lds_cptr p) {
    return __builtin_bit_cast(s16x4, __builtin_amdgcn_ds_read_tr16_b64_v4i16((__attribute__((address_space(3))) s16x4*)p));
}
__device__ __forceinline__ bf16x8 cat8(s16x4 a, s16x4 b) { return (bf16x8){a[0], a[1], a[2], a[3], b[0], b[1], b[2], b[3]}; }

__global__ __launch_bounds__(512, 2) void fused_gqa(const char* __restrict__ xA, const char* __restrict__ kA, const char* __restrict__ vA,
                                                    const char* __restrict__ wqA, const char* __restrict__ woA,
                                                    const char* __restrict__ xB, const char* __restrict__ kB, const char* __restrict__ vB,
                                                    const char* __restrict__ wqB, const char* __restrict__ woB, char* __restrict__ part,
                                                    unsigned short* __restrict__ out) {
    __shared__ __attribute__((aligned(1024))) char lds_raw[LDS_TOTAL];
    const int wid = __builtin_amdgcn_readfirstlane(threadIdx.x >> 6);
    const int wg = blockIdx.x, xg = wg & 7, br = xg >> 2, hg = xg & 3, b = wg >> 3;
    const unsigned lds0 = (unsigned)(uintptr_t)lds_raw;
    const lds_ptr L = (lds_ptr)lds_raw;
    PROF_DECL
    PRE(); POST();
    const char* xp = br ? xB : xA;
    const char* wq = br ? wqB : wqA;
    const char* wo = br ? woB : woA;

#define LOAD_X() do { \
        const int lane_ = lane_id(); \
        const unsigned voff = (unsigned)((lane_ & 15) * 1152 + (lane_ >> 4) * 16); \
        const char* xb = xp + (size_t)b * 64 * 1152; \
        _Pragma("unroll") for (int i = 0; i < 9; ++i) { \
            const int blk = wid * 9 + i, s = blk >> 2, m = blk & 3; \
            dma1(voff, xb + (m ^ hg) * 16 * 1152 + s * 64, lds0 + LDS_X + blk * 1024); \
        } } while (0)
    if (wid < 4) {
        unsigned pf0;
        {
            const int idx = wid * 18 + (lane_id() & 31);
            const int id2 = idx > 71 ? 71 : idx;
            const char* p0 = wq + (size_t)(18 * b + (id2 >> 2)) * 8192 + hg * 2048 + (id2 & 3) * 128;
            asm volatile("global_load_dword %0, %1, off" : "=&v"(pf0) : "v"(p0) : "memory");
        }
        LOAD_X();
        MARK();
        const int lane = lane_id();
        const int aw = wid, g = aw >> 1, half = aw & 1;
        const int r = lane & 31, h = lane >> 5;
        const char* kb = (br ? kB : kA) + (size_t)(b * 16 + 4 * hg) * 131072;
        const char* vb = (br ? vB : vA) + (size_t)(b * 16 + 4 * hg) * 131072;
        unsigned doff = (unsigned)((8 * aw + ((lane >> 2) & 3)) * 256 + ((lane >> 4) * 4 + ((lane & 3) ^ ((2 * aw) & 3))) * 16);
        unsigned doff1 = doff ^ 16;
        const unsigned ddst = lds0 + LDS_KV + aw * 2048;
        const u32x4 rK = mkrsrc(kb, 524288), rV = mkrsrc(vb - 24576, 524288 + 24576);
#define KVDMA(rs) asm volatile("buffer_load_dwordx4 %0, %2, 0 offen nt lds\n\tbuffer_load_dwordx4 %1, %2, 0 offen offset:1024 nt lds" ::"v"(doff), "v"(doff1), "s"(rs) : "memory")
#define ADV() do { doff += 8192; doff1 += 8192; PIN(doff); PIN(doff1); } while (0)
#define NOP0() asm volatile("s_nop 0")
        WAIT_BAR(0);
        SETM0_ADD(ddst, 0); NOP0(); KVDMA(rK); ADV();
        SETM0_ADD(ddst, 8192); NOP0(); KVDMA(rK); ADV();
        SETM0_ADD(ddst, 16384); NOP0(); KVDMA(rK); ADV();
        SETM0_ADD(ddst, 32768); NOP0(); KVDMA(rV);
        SETM0_ADD(ddst, 24576); NOP0(); KVDMA(rK); ADV();
        asm volatile("" ::"v"(pf0));
        int kb0 = LDS_KV + 2048 * (r >> 3) + 1024 * ((r >> 2) & 1) + 64 * (r & 3) + 16 * (h ^ ((r >> 2) & 3)), kb1 = kb0 ^ 32; PIN(kb0); PIN(kb1);
        const int qq = (lane & 15) >> 2, pp = lane & 3, gam = (lane >> 4) & 1;
        const int vbase = LDS_KV + 32768 + 1024 * h + 64 * qq + 16 * ((2 * gam + (pp >> 1)) ^ h) + 8 * (pp & 1);
        const int qoff = LDS_QO + (16 * g + 2 * half) * 1024 + lane * 16;
        const int ooff = LDS_QO + (16 * g + 2 * half + (r >> 4)) * 1024 + (r & 15) * 16 + 8 * h;
        f32x16 o[4], sA, sB = f32x16{}, negm = f32x16{};
        bf16x8 qf[8], kf[8];
        f32x2 ls2 = f32x2{};
        float mref = 0.f;
        int vr = 16384, vw = 8192;
        int vp0 = vbase + vr, vp1 = vp0 ^ 32;
#pragma unroll
        for (int i = 0; i < 4; ++i) o[i] = f32x16{};
#define VRD(i) do { vl[i] = vtr(L + vp0 + ((i) >> 2) * 4096 + ((i) & 3) * 256); vh[i] = vtr(L + vp1 + ((i) >> 2) * 4096 + 2048 + ((i) & 3) * 256); } while (0)
#define KRD(s, slot) kf[s] = ldsr128(L + (((s) & 1) ? kb1 : kb0) + (slot) * 8192 + ((s) >> 1) * 256)
        WAIT_BAR(8);
#pragma unroll
        for (int s = 0; s < 8; ++s) KRD(s, 0);
        WAIT_BAR(6);
#define EXP2(X, i) do { float e0_ = __builtin_amdgcn_exp2f(X[i]), e1_ = __builtin_amdgcn_exp2f(X[(i) + 1]); PIN(e0_); PIN(e1_); X[i] = e0_; X[(i) + 1] = e1_; } while (0)
#define SUM2(X, i) ls2 += (f32x2){X[i], X[(i) + 1]}
#define QK(C, s) do { SBAR(); C = MFMA32(kf[s], qf[s], C); SBAR(); } while (0)
#define PV(i, p) do { SBAR(); o[(i) & 3] = MFMA32(cat8(vl[i], vh[i]), __builtin_bit_cast(bf16x8, p), o[(i) & 3]); SBAR(); } while (0)
#define XMAX(x) ({ auto rr_ = __builtin_amdgcn_permlane32_swap(__float_as_uint(x), __float_as_uint(x), false, false); \
                   __builtin_fmaxf(__uint_as_float(rr_[0]), __uint_as_float(rr_[1])); })
#define STEP(C, P, PH, BOUND) do { \
            s16x4 vl[8], vh[8]; \
            if (BOUND) { _Pragma("unroll") for (int s = 0; s < 8; ++s) qf[s] = ldsr128(L + qoff + (s >> 1) * 4096 + (s & 1) * 1024); C = f32x16{}; QK(C, 0); } \
            else { SBAR(); asm volatile("v_mfma_f32_32x32x16_bf16 %0, %2, %3, %1" : "=&v"(C), "+v"(negm) : "v"(kf[0]), "v"(qf[0])); SBAR(); } \
            VRD(0); VRD(1); EXP2(P, 8); \
            QK(C, 1); VRD(2); VRD(3); EXP2(P, 10); \
            QK(C, 2); VRD(4); VRD(5); EXP2(P, 12); \
            { const unsigned m_ = ddst + 32768 + vw; SETM0(m_); } \
            QK(C, 3); VRD(6); VRD(7); EXP2(P, 14); \
            vw = vr; vr += 8192; asm volatile("" : "+s"(vr)); \
            QK(C, 4); KVDMA(rV); \
            u32x4 p0 = {cvtpk(P[0], P[1]), cvtpk(P[2], P[3]), cvtpk(P[4], P[5]), cvtpk(P[6], P[7])}; PIN(p0); \
            vr = (vr == 24576) ? 0 : vr; asm volatile("" : "+s"(vr)); \
            QK(C, 5); SUM2(P, 0); SUM2(P, 2); SUM2(P, 4); SUM2(P, 6); PIN(ls2); \
            SETM0_ADD(ddst, (PH) * 8192); \
            QK(C, 6); KVDMA(rK); \
            u32x4 p1 = {cvtpk(P[8], P[9]), cvtpk(P[10], P[11]), cvtpk(P[12], P[13]), cvtpk(P[14], P[15])}; PIN(p1); \
            QK(C, 7); \
            SUM2(P, 8); SUM2(P, 10); SUM2(P, 12); SUM2(P, 14); PIN(ls2); \
            LGKM0(); \
            PV(0, p0); KRD(0, ((PH) + 1) & 3); KRD(1, ((PH) + 1) & 3); ADV(); vp0 = vbase + vr; vp1 = vp0 ^ 32; PIN(vp0); PIN(vp1); \
            PV(1, p0); KRD(2, ((PH) + 1) & 3); KRD(3, ((PH) + 1) & 3); \
            float a_ = MX3(C[0], C[1], C[2]), c_ = MX3(C[3], C[4], C[5]); \
            a_ = MX3(a_, C[6], C[7]); c_ = MX3(c_, C[8], C[9]); PIN(a_); PIN(c_); \
            PV(2, p0); KRD(4, ((PH) + 1) & 3); KRD(5, ((PH) + 1) & 3); \
            float d_ = MX3(C[10], C[11], C[12]), e_ = MX3(C[13], C[14], C[15]); \
            asm volatile("v_max3_f32 %0, %0, %1, %2\n\tv_max_f32 %0, %0, %3" : "+v"(a_) : "v"(c_), "v"(d_), "v"(e_)); \
            PV(3, p0); KRD(6, ((PH) + 1) & 3); KRD(7, ((PH) + 1) & 3); \
            float lprev = 0.f; \
            if (BOUND) { \
                lprev = ls2[0] + ls2[1]; ls2 = f32x2{}; \
                const float rm = XMAX(a_); mref = rm; \
                _Pragma("unroll") for (int i = 0; i < 16; ++i) { C[i] -= rm; negm[i] = -rm; } \
                PIN(negm); \
            } else if (__builtin_expect(__builtin_amdgcn_ballot_w64(a_ > THR) != 0, 0)) { \
                  \
                const float dl = __builtin_fmaxf(__builtin_ceilf(XMAX(a_)), 0.f); \
                const float al = __builtin_amdgcn_exp2f(-dl); mref += dl; ls2 *= al; \
                _Pragma("unroll") for (int i = 0; i < 16; ++i) { C[i] -= dl; negm[i] = -mref; } \
                PIN(negm); \
                _Pragma("unroll") for (int i = 0; i < 4; ++i) p1[i] = cvtpk(__uint_as_float(p1[i] << 16) * al, __uint_as_float(p1[i] & 0xffff0000u) * al); \
                _Pragma("unroll") for (int tt = 0; tt < 4; ++tt) \
                    _Pragma("unroll") for (int i = 0; i < 16; ++i) o[tt][i] *= al; \
                PIN(C); PIN(p1); PIN(ls2); PIN(o[0]); PIN(o[1]); PIN(o[2]); PIN(o[3]); \
            } \
            PV(4, p1); EXP2(C, 0); PV(5, p1); EXP2(C, 2); PV(6, p1); EXP2(C, 4); PV(7, p1); EXP2(C, 6); SBAR(); \
            if (BOUND) { \
                if (u > 0) { \
                    auto r2_ = __builtin_amdgcn_permlane32_swap(__float_as_uint(lprev), __float_as_uint(lprev), false, false); \
                    const float inv = 1.0f / (__uint_as_float(r2_[0]) + __uint_as_float(r2_[1])); \
                    _Pragma("unroll") for (int tt = 0; tt < 4; ++tt) \
                        _Pragma("unroll") for (int k = 0; k < 4; ++k) { \
                            u32x2 w = {cvtpk(o[tt][4 * k] * inv, o[tt][4 * k + 1] * inv), cvtpk(o[tt][4 * k + 2] * inv, o[tt][4 * k + 3] * inv)}; \
                            *(__attribute__((address_space(3))) u32x2*)(L + ooff + tt * 4096 + k * 256) = w; \
                        } \
                } \
                _Pragma("unroll") for (int i = 0; i < 4; ++i) o[i] = f32x16{}; \
            } \
            WAIT_BAR(6); \
        } while (0)
        for (int u = 0; u < 4; ++u) {
            STEP(sA, sB, 0, true); STEP(sB, sA, 1, false); STEP(sA, sB, 2, false); STEP(sB, sA, 3, false);
            for (int j = 0; j < 3; ++j) {
                STEP(sA, sB, 0, false); STEP(sB, sA, 1, false); STEP(sA, sB, 2, false); STEP(sB, sA, 3, false);
            }
        }
        { const int u = 4; STEP(sA, sB, 0, true); }
        WAITVM(0);
        PROF_END();
        __builtin_amdgcn_endpgm();
    } else {
        const int pw = wid - 4;
        unsigned wv0, wv1;
        const u32x4 rW = mkrsrc(wq + hg * 2048 + pw * 128, 576 * 8192);
        const unsigned wdst = lds0 + LDS_QO + pw * 8192;
        unsigned wp;
#define SET_W(u) do { const int l_ = lane_id(); wv0 = (unsigned)((l_ >> 3) * 8192 + (((l_ & 7) ^ (((l_ >> 4) & 1) << 2)) * 16)); wv1 = wv0 + (65536 - 1024); \
                      wp = (u) * 512; asm volatile("" : "+s"(wp)); } while (0)
#define ISSUE_W(i) do { SETM0_ADD(wdst, ((i) & 3) * 2048); asm volatile("s_nop 0"); BDMA2(wv0, wv1, rW, wp); wp += 131072; asm volatile("" : "+s"(wp)); } while (0)
#define START_W(u) do { SET_W(u); ISSUE_W(0); ISSUE_W(1); ISSUE_W(2); ISSUE_W(3); } while (0)
        const auto ro = __builtin_amdgcn_make_buffer_rsrc((void*)wo, 0, 4096 * 1152, 0x00020000);
        f32x4 acc[9][4];
        int so2, sp, sp2, so;
        START_W(0);
        LOAD_X();
        MARK();
        WAIT_BAR(0);
#define RD_A(k, buf) do { \
            af[buf][0] = cat8(vtr(L + wbase + ((k) & 3) * 2048), vtr(L + wbase + ((k) & 3) * 2048 + 512)); \
            af[buf][1] = cat8(vtr(L + wb1 + ((k) & 3) * 2048), vtr(L + wb1 + ((k) & 3) * 2048 + 512)); } while (0)
#define RD_B(k, buf) do { \
            if ((k) == 32) { xbase += 65536; PIN(xbase); } \
            bfr[buf][0] = ldsr128(L + xbase + (((k) >> 1) & 15) * 4096 + ((k) & 1) * 512); \
            bfr[buf][1] = ldsr128(L + xbase + (((k) >> 1) & 15) * 4096 + ((k) & 1) * 512 + 2048); } while (0)
#define QM(i, j, k) do { SBAR(); qa[i][j] = MFMA32(af[(k) & 1][i], bfr[(k) & 1][j], qa[i][j]); PIN(qa[i][j]); SBAR(); } while (0)
#define Q_PHASE(BARS) do { \
            const int lane = lane_id(), r = lane & 31, h = lane >> 5, qq = (lane & 15) >> 2, pp = lane & 3, gam = (lane >> 4) & 1; \
            int wbase = LDS_QO + pw * 8192 + 128 * (8 * h + qq) + 16 * (2 * gam + (pp & 1)) + 8 * (pp >> 1) + 64 * (qq >> 1); PIN(wbase); \
            int xbase = LDS_X + (r >> 4) * 1024 + h * 256 + (r & 15) * 16; PIN(xbase); \
            f32x16 qa[2][2]; \
            qa[0][0] = f32x16{}; qa[0][1] = f32x16{}; qa[1][0] = f32x16{}; qa[1][1] = f32x16{}; \
            bf16x8 af[2][2], bfr[2][2]; \
            int wb1 = wbase ^ 64; PIN(wb1); \
            WAITVM(6); \
            RD_A(0, 0); RD_B(0, 0); \
            _Pragma("unroll") for (int k = 0; k < 36; ++k) { \
                if (k + 4 < 36) SETM0_ADD(wdst, (k & 3) * 2048); \
                LGKM0(); \
                QM(0, 0, k); \
                if (k + 4 < 36) { BDMA2(wv0, wv1, rW, wp); wp += 131072; asm volatile("" : "+s"(wp)); } \
                if (k < 35) RD_B(k + 1, (k + 1) & 1); \
                if (BARS) PACE(QP - 24); \
                QM(0, 1, k); \
                if (k < 35) { \
                    if (k < 32) WAITVM(6); else if (k == 32) WAITVM(4); else if (k == 33) WAITVM(2); else WAITVM(0); \
                    RD_A(k + 1, (k + 1) & 1); \
                } \
                if (BARS) PACE(QP - 20); \
                QM(1, 0, k); \
                if (BARS) PACE(QP); \
                QM(1, 1, k); \
                if (BARS) PACE(QP - 8); \
                if (!(BARS) && k == 30) BAR(); \
                if (BARS && k <= 30 && k % 5 == 0) BAR(); \
            } \
            const int qwbase = LDS_QO + pw * 8192 + lane_id() * 16; \
            _Pragma("unroll") for (int t = 0; t < 2; ++t) \
                _Pragma("unroll") for (int m = 0; m < 2; ++m) \
                    _Pragma("unroll") for (int k = 0; k < 2; ++k) { \
                        const f32x16& a = qa[t][m]; \
                        u32x4 w = {cvtpk(a[8 * k] * QSCALE, a[8 * k + 1] * QSCALE), cvtpk(a[8 * k + 2] * QSCALE, a[8 * k + 3] * QSCALE), \
                                   cvtpk(a[8 * k + 4] * QSCALE, a[8 * k + 5] * QSCALE), cvtpk(a[8 * k + 6] * QSCALE, a[8 * k + 7] * QSCALE)}; \
                        *(__attribute__((address_space(3))) u32x4*)(L + qwbase + t * 4096 + m * 2048 + k * 1024) = w; \
                    } \
            SBAR(); \
        } while (0)
#define LOADJ(set, j) do { if ((j) < 8) raw[set][(j) & 7] = __builtin_amdgcn_raw_buffer_load_b128(ro, voa + ((j) & 3) * 1152, ((j) & 4) ? so2 : so, 0); \
                           else rc[(j) & 3] = __builtin_amdgcn_raw_buffer_load_b32(ro, voc + ((j) & 1) * 2304, ((j) & 2) ? sp2 : sp, 0); } while (0)
#define NEXT_ROWS() do { sp = so; sp2 = so2; so += 32 * 1152; asm volatile("" : "+s"(so)); so2 = so + 4608; asm volatile("" : "+s"(so2)); } while (0)
#define PERM1(dst, set, t, j) do { \
            if ((t) < 8) dst[j] = __builtin_amdgcn_perm(raw[set][2 * (j) + 1][((t) >> 1) & 3], raw[set][2 * (j)][((t) >> 1) & 3], ((t) & 1) ? 0x07060302u : 0x05040100u); \
            else dst[j] = __builtin_amdgcn_perm((unsigned)__builtin_amdgcn_mov_dpp((int)rc[j], 0xB1, 0xf, 0xf, true), rc[j], selc); } while (0)
#define O_PHASE(BARS, NEXT) do { \
            const int lane = lane_id(), c = lane & 15, q = lane >> 4; \
            int obase = LDS_QO + lane * 16; PIN(obase); \
            const int voa = (1024 * hg + 8 * q) * 1152 + (128 * pw + 8 * c) * 2; \
            const int voc = (1024 * hg + 8 * q + (c & 1)) * 1152 + (512 + 16 * pw + (c & 14)) * 2; \
            const unsigned selc = (c & 1) ? 0x03020706u : 0x05040100u; \
            bf16x8 of[4]; u32x4 ft[2], raw[2][8]; unsigned rc[4]; \
            _Pragma("unroll") for (int j = 0; j < 8; ++j) LOADJ(0, j); \
            NEXT_ROWS(); \
            _Pragma("unroll") for (int j = 0; j < 12; ++j) LOADJ(1, j); \
            NEXT_ROWS(); \
            SBAR(); \
            if (!(BARS)) { for (int j = 0; j < 8; ++j) BAR(); } \
            BAR(); \
            _Pragma("unroll") for (int m = 0; m < 4; ++m) of[m] = ldsr128(L + obase + m * 1024); \
            _Pragma("unroll") for (int j = 0; j < 4; ++j) PERM1(ft[0], 0, 0, j); \
            PIN(ft[0]); asm volatile("s_nop 1"); \
            _Pragma("unroll") for (int s = 0; s < 8; ++s) { \
                if (s == 6 && BARS) { SET_W(NEXT); if ((NEXT) >= 0) { ISSUE_W(0); ISSUE_W(1); } } \
                if (s == 7 && (NEXT) >= 0) { ISSUE_W(2); ISSUE_W(3); } \
                _Pragma("unroll") for (int t = 0; t < 9; ++t) \
                    _Pragma("unroll") for (int m = 0; m < 4; ++m) { \
                        const int g = s * 9 + t, nset = (t < 8) ? (s & 1) : ((s + 1) & 1), nt = (t < 8) ? t + 1 : 0, pos = t * 4 + m - 26; \
                        SBAR(); \
                        MFMA16_ACC(acc[t][m], ft[g & 1], of[m]); \
                        SBAR(); \
                        if (PPOS == 0 && m < 2 && g < 71) { PERM1(ft[(g + 1) & 1], nset, nt, 2 * m); PERM1(ft[(g + 1) & 1], nset, nt, 2 * m + 1); PIN(ft[(g + 1) & 1]); } \
                        if (PPOS == 1 && m == 1 && g < 71) { PERM1(ft[(g + 1) & 1], nset, nt, 0); PERM1(ft[(g + 1) & 1], nset, nt, 1); PERM1(ft[(g + 1) & 1], nset, nt, 2); PERM1(ft[(g + 1) & 1], nset, nt, 3); PIN(ft[(g + 1) & 1]); } \
                        if (s < 6 && pos >= 0 && pos % LSTEP == 0) LOADJ(s & 1, pos / LSTEP); \
                        if (s < 7 && pos >= 4 && (pos - 4) % LSTEP == 0 && (pos - 4) / LSTEP < 4) LOADJ(0, 8 + (pos - 4) / LSTEP); \
                        if (s > 0 && s < 7 && (pos + 36) % LSTEP == 0 && (pos + 36) / LSTEP < 8) LOADJ((s - 1) & 1, (pos + 36) / LSTEP); \
                        if (s > 0 && (pos + 32) % LSTEP == 0 && (pos + 32) / LSTEP < 4) LOADJ(0, 8 + (pos + 32) / LSTEP); \
                        if (s > 0 && s < 7 && pos == -4) NEXT_ROWS(); \
                        if (t == 8 && s < 7) of[m] = ldsr128(L + obase + (s + 1) * 4096 + m * 1024); \
                    } \
                SBAR(); \
                if (BARS && s < 7) LGKM_BAR(); \
                if (!(BARS)) MARK(); \
            } \
        } while (0)
        Q_PHASE(false);
        LGKM_BAR();
        BAR();
        {
            const int idx = pw * 64 + lane_id();
            const char* q0 = wq + (size_t)(18 * b) * 8192 + hg * 2048 + 512;
            const char* o0 = wo + (size_t)(1024 * hg + 32 * b) * 1152;
            const int i12 = idx / 12;
            const char* p0 = idx < 216 ? q0 + i12 * 8192 + (idx - 12 * i12) * 128 : o0 + (idx & 31) * 128;
            const char* p1 = o0 + 4096 + idx * 128;
            unsigned pf1, pf2;
            asm volatile("global_load_dword %0, %2, off\n\tglobal_load_dword %1, %3, off" : "=&v"(pf1), "=&v"(pf2) : "v"(p0), "v"(p1) : "memory");
            for (int j = 0; j < 6; ++j) BAR();
            WAITVM(0);
            asm volatile("" ::"v"(pf1), "v"(pf2));
        }
        START_W(1);
        BAR();
#pragma unroll
        for (int t = 0; t < 9; ++t)
#pragma unroll
            for (int m = 0; m < 4; ++m) acc[t][m] = f32x4{};
        so = 0; so2 = 4608; sp = 0; sp2 = 0;
        for (int i = 1; i < 4; ++i) {
            Q_PHASE(true);
            LGKM_BAR();
            O_PHASE(true, (i < 3 ? i + 1 : -1));
        }
        O_PHASE(false, -1);
        asm volatile("s_nop 15\n\ts_nop 15");
        const int lane = lane_id();
        RT(0);
        const auto rx = __builtin_amdgcn_make_buffer_rsrc((void*)part, 0, XCH_BYTES, 0x00020000);
        gu32* flags = (gu32*)(part + FLAG_OFF);
#pragma unroll
        for (int j = 1; j < 4; ++j) {
            const int sb = (((wg ^ j) * 4 + pw) * 3 + j - 1) * 4608;
#pragma unroll
            for (int i = 0; i < 4; ++i) {
                const f32x4 &a0 = acc[2 * i][j], &a1 = acc[2 * i + 1][j];
                u32x4 w = {cvtpk(a0[0], a0[1]), cvtpk(a0[2], a0[3]), cvtpk(a1[0], a1[1]), cvtpk(a1[2], a1[3])};
                __builtin_amdgcn_raw_buffer_store_b128(w, rx, lane * 16 + i * 1024, sb, 16);
            }
            u32x2 w = {cvtpk(acc[8][j][0], acc[8][j][1]), cvtpk(acc[8][j][2], acc[8][j][3])};
            __builtin_amdgcn_raw_buffer_store_b64(w, rx, lane * 8 + 4096, sb, 16);
        }
        WAITVM(0);
        asm volatile("s_barrier" ::: "memory");
        RT(1);
        gu32* fl = flags + (wg >> 2) * 64;
        if (pw == 0 && lane == 0) __hip_atomic_store(fl + hg, 1u, __ATOMIC_RELAXED, __HIP_MEMORY_SCOPE_AGENT);
        for (unsigned spins = 0; spins < (1u << 17); ++spins) {
            const unsigned v = __hip_atomic_load(fl + (lane & 3), __ATOMIC_RELAXED, __HIP_MEMORY_SCOPE_AGENT);
            if (__builtin_amdgcn_ballot_w64(v != 1u) == 0) break;
            __builtin_amdgcn_s_sleep(1);
        }
        __builtin_amdgcn_fence(__ATOMIC_ACQUIRE, "wavefront");
        asm volatile("" ::: "memory");
        RT(2);
        u32x4 rv[3][4]; u32x2 rw[3];
#pragma unroll
        for (int j = 0; j < 3; ++j) {
            const int sb = ((wg * 4 + pw) * 3 + j) * 4608;
#pragma unroll
            for (int i = 0; i < 4; ++i) rv[j][i] = __builtin_amdgcn_raw_buffer_load_b128(rx, lane * 16 + i * 1024, sb, 16);
            rw[j] = __builtin_amdgcn_raw_buffer_load_b64(rx, lane * 8 + 4096, sb, 16);
        }
#define BLO(x) __uint_as_float((x) << 16)
#define BHI(x) __uint_as_float((x) & 0xffff0000u)
#pragma unroll
        for (int j = 0; j < 3; ++j) {
#pragma unroll
            for (int i = 0; i < 4; ++i) {
                acc[2 * i][0] += (f32x4){BLO(rv[j][i][0]), BHI(rv[j][i][0]), BLO(rv[j][i][1]), BHI(rv[j][i][1])};
                acc[2 * i + 1][0] += (f32x4){BLO(rv[j][i][2]), BHI(rv[j][i][2]), BLO(rv[j][i][3]), BHI(rv[j][i][3])};
            }
            acc[8][0] += (f32x4){BLO(rw[j][0]), BHI(rw[j][0]), BLO(rw[j][1]), BHI(rw[j][1])};
        }
        RT(3);
        unsigned short* orow = out + ((size_t)(b * 64 + 16 * hg + (lane & 15)) * 1152 + br * 576);
        const int q = lane >> 4;
#pragma unroll
        for (int r = 0; r < 4; ++r) {
            u32x4 w = {cvtpk(acc[0][0][r], acc[1][0][r]), cvtpk(acc[2][0][r], acc[3][0][r]), cvtpk(acc[4][0][r], acc[5][0][r]), cvtpk(acc[6][0][r], acc[7][0][r])};
            *(u32x4*)(orow + 128 * pw + 32 * q + 8 * r) = w;
        }
        u32x2 w = {cvtpk(acc[8][0][0], acc[8][0][1]), cvtpk(acc[8][0][2], acc[8][0][3])};
        *(u32x2*)(orow + 512 + 16 * pw + 4 * q) = w;
        PROF_END();
        __builtin_amdgcn_endpgm();
    }
}

__global__ __launch_bounds__(64) void zero_flags(char* __restrict__ part) {
    *(u32x4*)(part + FLAG_OFF + threadIdx.x * 256) = u32x4{};
}

extern "C" void kernel_launch(void* const* d_in, const int* in_sizes, int n_in, void* d_out, int out_size, void* d_ws, size_t ws_size,
                              hipStream_t stream) {
    (void)in_sizes; (void)n_in; (void)out_size; (void)ws_size;
    char* part = (char*)d_ws;
    zero_flags<<<1, 64, 0, stream>>>(part);
    fused_gqa<<<256, 512, 0, stream>>>((const char*)d_in[0], (const char*)d_in[1], (const char*)d_in[2], (const char*)d_in[3], (const char*)d_in[4],
                                       (const char*)d_in[5], (const char*)d_in[6], (const char*)d_in[7], (const char*)d_in[8], (const char*)d_in[9], part,
                                       (unsigned short*)d_out);
}
```

```cpp
#include <hip/hip_runtime.h>
#include <hip/hip_bf16.h>
#include <stdint.h>

typedef short bf16x8 __attribute__((ext_vector_type(8)));
typedef short s16x4 __attribute__((ext_vector_type(4)));
typedef float f32x16 __attribute__((ext_vector_type(16)));
typedef float f32x4 __attribute__((ext_vector_type(4)));
typedef float f32x2 __attribute__((ext_vector_type(2)));
typedef unsigned u32x4 __attribute__((ext_vector_type(4)));
typedef unsigned u32x2 __attribute__((ext_vector_type(2)));
typedef __attribute__((address_space(3))) char* lds_ptr;
typedef __attribute__((address_space(3))) const char* lds_cptr;

constexpr int LDS_X = 0, LDS_QO = 73728, LDS_KV = 106496, LDS_TOTAL = 163840;
constexpr float QSCALE = 0.08838834764831845f * 1.4426950408889634f;
constexpr float THR = 8.0f;
constexpr int XCH_BYTES = 256 * 4 * 3 * 4608, FLAG_OFF = 16 << 20, FLAG_BYTES = 64 * 256;
typedef __attribute__((address_space(1))) unsigned gu32;
#define LSTEP 2
#define QP 0
#define PPOS 0
#define PACE(n) do { if ((n) > 0) asm volatile("s_nop %0" ::"n"((n) > 16 ? 15 : (n) - 1)); if ((n) > 16) asm volatile("s_nop %0" ::"n"((n) > 32 ? 15 : (n) - 17)); if ((n) > 32) asm volatile("s_nop %0" ::"n"((n) - 33)); } while (0)

#define PFS 2
#define SBAR() __builtin_amdgcn_sched_barrier(0)
#define PIN(x) asm volatile("" : "+v"(x))
#define MFMA32(a, b, c) __builtin_amdgcn_mfma_f32_32x32x16_bf16(a, b, c, 0, 0, 0)
#define MFMA16(a, b, c) __builtin_amdgcn_mfma_f32_16x16x32_bf16(a, b, c, 0, 0, 0)
#define MFMA16_ACC(c, a, b) asm volatile("v_mfma_f32_16x16x32_bf16 %0, %1, %2, %0" : "+v"(c) : "v"(a), "v"(b))
#define PRE()
#define POST()
#define PROF_DECL
#define RT(i)
#define PROF_END()
#define WAITC(vm, lg) __builtin_amdgcn_s_waitcnt(((vm) & 15) | 0x70 | ((lg) << 8) | (((vm) >> 4) << 14))
#define WAIT_BAR(N) do { PRE(); asm volatile("" ::: "memory"); WAITC(N, 0); asm volatile("s_barrier" ::: "memory"); POST(); } while (0)
#define LGKM_BAR() WAIT_BAR(63)
#define LGKM0() do { asm volatile("" ::: "memory"); WAITC(63, 0); asm volatile("" ::: "memory"); } while (0)
#define BAR() do { PRE(); asm volatile("s_barrier" ::: "memory"); POST(); } while (0)
#define MARK() do { PRE(); POST(); } while (0)
#define WAITVM(N) asm volatile("s_waitcnt vmcnt(" #N ")" ::: "memory")
#define MX3(a, b, c) __builtin_fmaxf(__builtin_fmaxf((a), (b)), (c))

__device__ __forceinline__ unsigned cvtpk(float lo, float hi) {
    typedef float f32x2_t __attribute__((ext_vector_type(2)));
    typedef __bf16 bf16x2_t __attribute__((ext_vector_type(2)));
    return __builtin_bit_cast(unsigned, __builtin_convertvector((f32x2_t){lo, hi}, bf16x2_t));
}
__device__ __forceinline__ void dma1(unsigned voff, const char* sbase, unsigned lds_base) {
    unsigned sv;
    asm volatile("s_mov_b32 %0, m0\n\ts_mov_b32 m0, %3\n\ts_nop 0\n\tglobal_load_lds_dwordx4 %1, %2\n\ts_mov_b32 m0, %0"
                 : "=&s"(sv) : "v"(voff), "s"(sbase), "s"(lds_base) : "memory");
}
#define SETM0(v) asm volatile("s_mov_b32 m0, %0" ::"s"(v) : "memory")
#define SETM0_ADD(v, imm) asm volatile("s_add_u32 m0, %0, %1" ::"s"(v), "n"(imm) : "memory", "scc")
#define BDMA2NT(voff, rs) asm volatile("buffer_load_dwordx4 %0, %1, 0 offen nt lds\n\tbuffer_load_dwordx4 %0, %1, 0 offen offset:1024 nt lds" ::"v"(voff), "s"(rs) : "memory")
#define BDMA2(v0, v1, rs, so) asm volatile("buffer_load_dwordx4 %0, %2, %3 offen lds\n\tbuffer_load_dwordx4 %1, %2, %3 offen offset:1024 lds" ::"v"(v0), "v"(v1), "s"(rs), "s"(so) : "memory")
__device__ __forceinline__ u32x4 mkrsrc(const char* p, unsigned n) {
    const unsigned long long a = (unsigned long long)(uintptr_t)p;
    return (u32x4){(unsigned)a, (unsigned)(a >> 32) & 0xffffu, n, 0x00020000u};
}
__device__ __forceinline__ int lane_id() { int l; asm volatile("v_mbcnt_lo_u32_b32 %0, -1, 0\n\tv_mbcnt_hi_u32_b32 %0, -1, %0" : "=v"(l)); return l; }
__device__ __forceinline__ bf16x8 ldsr128(lds_cptr p) { return *(const __attribute__((address_space(3))) bf16x8*)p; }
__device__ __forceinline__ s16x4 vtr(lds_cptr p) {
    return __builtin_bit_cast(s16x4, __builtin_amdgcn_ds_read_tr16_b64_v4i16((__attribute__((address_space(3))) s16x4*)p));
}
__device__ __forceinline__ bf16x8 cat8(s16x4 a, s16x4 b) { return (bf16x8){a[0], a[1], a[2], a[3], b[0], b[1], b[2], b[3]}; }

__global__ __launch_bounds__(512, 2) void fused_gqa(const char* __restrict__ xA, const char* __restrict__ kA, const char* __restrict__ vA,
                                                    const char* __restrict__ wqA, const char* __restrict__ woA,
                                                    const char* __restrict__ xB, const char* __restrict__ kB, const char* __restrict__ vB,
                                                    const char* __restrict__ wqB, const char* __restrict__ woB, char* __restrict__ part,
                                                    unsigned short* __restrict__ out) {
    __shared__ __attribute__((aligned(1024))) char lds_raw[LDS_TOTAL];
    const int wid = __builtin_amdgcn_readfirstlane(threadIdx.x >> 6);
    const int wg = blockIdx.x, xg = wg & 7, br = xg >> 2, hg = xg & 3, b = wg >> 3;
    const unsigned lds0 = (unsigned)(uintptr_t)lds_raw;
    const lds_ptr L = (lds_ptr)lds_raw;
    PROF_DECL
    PRE(); POST();
    const char* xp = br ? xB : xA;
    const char* wq = br ? wqB : wqA;
    const char* wo = br ? woB : woA;

#define LOAD_X() do { \
        const int lane_ = lane_id(); \
        const unsigned voff = (unsigned)(((lane_ >> 2) & 7) * 1152 + (lane_ >> 5) * 64 + (((lane_ & 3) ^ ((lane_ >> 4) & 1) ^ ((wid & 1) << 1)) << 4)); \
        const char* xb = xp + (size_t)(b * 64 + 8 * (wid ^ (2 * hg))) * 1152; \
        _Pragma("unroll") for (int i = 0; i < 9; ++i) dma1(voff, xb + i * 128, lds0 + LDS_X + i * 8192 + wid * 1024); \
        } while (0)
    if (wid < 4) {
        unsigned pf0;
        {
            const int idx = wid * 18 + (lane_id() & 31);
            const int id2 = idx > 71 ? 71 : idx;
            const char* p0 = wq + (size_t)(18 * b + (id2 >> 2)) * 8192 + hg * 2048 + (id2 & 3) * 128;
            asm volatile("global_load_dword %0, %1, off" : "=&v"(pf0) : "v"(p0) : "memory");
        }
        LOAD_X();
        MARK();
        const int lane = lane_id();
        const int aw = wid, g = aw >> 1, half = aw & 1;
        const int r = lane & 31, h = lane >> 5;
        const char* kb = (br ? kB : kA) + (size_t)(b * 16 + 4 * hg) * 131072;
        const char* vb = (br ? vB : vA) + (size_t)(b * 16 + 4 * hg) * 131072;
        unsigned doff = (unsigned)((8 * aw + ((lane >> 2) & 3)) * 256 + ((lane >> 4) * 4 + ((lane & 3) ^ ((2 * aw) & 3))) * 16);
        unsigned doff1 = doff ^ 16;
        const unsigned ddst = lds0 + LDS_KV + aw * 2048;
        const u32x4 rK = mkrsrc(kb, 524288), rV = mkrsrc(vb - 24576, 524288 + 24576);
#define KVDMA(rs) asm volatile("buffer_load_dwordx4 %0, %2, 0 offen nt lds\n\tbuffer_load_dwordx4 %1, %2, 0 offen offset:1024 nt lds" ::"v"(doff), "v"(doff1), "s"(rs) : "memory")
#define ADV() do { doff += 8192; doff1 += 8192; PIN(doff); PIN(doff1); } while (0)
#define NOP0() asm volatile("s_nop 0")
        WAIT_BAR(0);
        SETM0_ADD(ddst, 0); NOP0(); KVDMA(rK); ADV();
        SETM0_ADD(ddst, 8192); NOP0(); KVDMA(rK); ADV();
        SETM0_ADD(ddst, 16384); NOP0(); KVDMA(rK); ADV();
        SETM0_ADD(ddst, 32768); NOP0(); KVDMA(rV);
        SETM0_ADD(ddst, 24576); NOP0(); KVDMA(rK); ADV();
        asm volatile("" ::"v"(pf0));
        int kb0 = LDS_KV + 2048 * (r >> 3) + 1024 * ((r >> 2) & 1) + 64 * (r & 3) + 16 * (h ^ ((r >> 2) & 3)), kb1 = kb0 ^ 32; PIN(kb0); PIN(kb1);
        const int qq = (lane & 15) >> 2, pp = lane & 3, gam = (lane >> 4) & 1;
        const int vbase = LDS_KV + 32768 + 1024 * h + 64 * qq + 16 * ((2 * gam + (pp >> 1)) ^ h) + 8 * (pp & 1);
        const int qoff = LDS_QO + (16 * g + 2 * half) * 1024 + lane * 16;
        const int ooff = LDS_QO + (16 * g + 2 * half + (r >> 4)) * 1024 + (r & 15) * 16 + 8 * h;
        const char* pfb = aw < 2 ? wq + (size_t)(18 * b + 9 * aw) * 8192 + hg * 2048 + 1024 : wo + (size_t)hg * 1179648 + (18 * b + 9 * (aw - 2)) * 128;
        const int pfs = aw < 2 ? 13 : 7;
        const char* pfe = aw < 2 ? pfb + 896 : pfb + 15 * 73728;
        f32x16 o[4], sA, sB = f32x16{}, negm = f32x16{};
        bf16x8 qf[8], kf[8];
        f32x2 ls2 = f32x2{};
        float mref = 0.f;
        int vr = 16384, vw = 8192;
        int vp0 = vbase + vr, vp1 = vp0 ^ 32;
#pragma unroll
        for (int i = 0; i < 4; ++i) o[i] = f32x16{};
#define VRD(i) do { vl[i] = vtr(L + vp0 + ((i) >> 2) * 4096 + ((i) & 3) * 256); vh[i] = vtr(L + vp1 + ((i) >> 2) * 4096 + 2048 + ((i) & 3) * 256); } while (0)
#define KRD(s, slot) kf[s] = ldsr128(L + (((s) & 1) ? kb1 : kb0) + (slot) * 8192 + ((s) >> 1) * 256)
        WAIT_BAR(8);
#pragma unroll
        for (int s = 0; s < 8; ++s) KRD(s, 0);
        WAIT_BAR(6);
#define EXP2(X, i) do { float e0_ = __builtin_amdgcn_exp2f(X[i]), e1_ = __builtin_amdgcn_exp2f(X[(i) + 1]); PIN(e0_); PIN(e1_); X[i] = e0_; X[(i) + 1] = e1_; } while (0)
#define SUM2(X, i) ls2 += (f32x2){X[i], X[(i) + 1]}
#define QK(C, s) do { SBAR(); C = MFMA32(kf[s], qf[s], C); SBAR(); } while (0)
#define PV(i, p) do { SBAR(); o[(i) & 3] = MFMA32(cat8(vl[i], vh[i]), __builtin_bit_cast(bf16x8, p), o[(i) & 3]); SBAR(); } while (0)
#define XMAX(x) ({ auto rr_ = __builtin_amdgcn_permlane32_swap(__float_as_uint(x), __float_as_uint(x), false, false); \
                   __builtin_fmaxf(__uint_as_float(rr_[0]), __uint_as_float(rr_[1])); })
#define STEP(C, P, PH, BOUND, PF) do { \
            s16x4 vl[8], vh[8]; \
            if (BOUND) { _Pragma("unroll") for (int s = 0; s < 8; ++s) qf[s] = ldsr128(L + qoff + (s >> 1) * 4096 + (s & 1) * 1024); C = f32x16{}; \
                QK(C, 0); } \
            else { SBAR(); asm volatile("v_mfma_f32_32x32x16_bf16 %0, %2, %3, %1" : "=&v"(C), "+v"(negm) : "v"(kf[0]), "v"(qf[0])); SBAR(); } \
            VRD(0); VRD(1); EXP2(P, 8); \
            QK(C, 1); VRD(2); VRD(3); EXP2(P, 10); \
            QK(C, 2); VRD(4); VRD(5); EXP2(P, 12); \
            { const unsigned m_ = ddst + 32768 + vw; SETM0(m_); } \
            QK(C, 3); VRD(6); VRD(7); EXP2(P, 14); \
            vw = vr; vr += 8192; asm volatile("" : "+s"(vr)); \
            QK(C, 4); KVDMA(rV); \
            u32x4 p0 = {cvtpk(P[0], P[1]), cvtpk(P[2], P[3]), cvtpk(P[4], P[5]), cvtpk(P[6], P[7])}; PIN(p0); \
            vr = (vr == 24576) ? 0 : vr; asm volatile("" : "+s"(vr)); \
            QK(C, 5); SUM2(P, 0); SUM2(P, 2); SUM2(P, 4); SUM2(P, 6); PIN(ls2); \
            SETM0_ADD(ddst, (PH) * 8192); \
            QK(C, 6); \
            if (PF) { const unsigned po_ = min(lane_id(), 8) << pfs; \
                asm volatile("global_load_lds_dword %0, %1" ::"v"(po_), "s"(pfb) : "memory");     \
                pfb += aw < 2 ? 128 : 73728; pfb = pfb > pfe ? pfe : pfb; } \
            KVDMA(rK); \
            u32x4 p1 = {cvtpk(P[8], P[9]), cvtpk(P[10], P[11]), cvtpk(P[12], P[13]), cvtpk(P[14], P[15])}; PIN(p1); \
            QK(C, 7); \
            SUM2(P, 8); SUM2(P, 10); SUM2(P, 12); SUM2(P, 14); PIN(ls2); \
            LGKM0(); \
            PV(0, p0); KRD(0, ((PH) + 1) & 3); KRD(1, ((PH) + 1) & 3); ADV(); vp0 = vbase + vr; vp1 = vp0 ^ 32; PIN(vp0); PIN(vp1); \
            PV(1, p0); KRD(2, ((PH) + 1) & 3); KRD(3, ((PH) + 1) & 3); \
            float a_ = MX3(C[0], C[1], C[2]), c_ = MX3(C[3], C[4], C[5]); \
            a_ = MX3(a_, C[6], C[7]); c_ = MX3(c_, C[8], C[9]); PIN(a_); PIN(c_); \
            PV(2, p0); KRD(4, ((PH) + 1) & 3); KRD(5, ((PH) + 1) & 3); \
            float d_ = MX3(C[10], C[11], C[12]), e_ = MX3(C[13], C[14], C[15]); \
            asm volatile("v_max3_f32 %0, %0, %1, %2\n\tv_max_f32 %0, %0, %3" : "+v"(a_) : "v"(c_), "v"(d_), "v"(e_)); \
            PV(3, p0); KRD(6, ((PH) + 1) & 3); KRD(7, ((PH) + 1) & 3); \
            float lprev = 0.f; \
            if (BOUND) { \
                lprev = ls2[0] + ls2[1]; ls2 = f32x2{}; \
                const float rm = XMAX(a_); mref = rm; \
                _Pragma("unroll") for (int i = 0; i < 16; ++i) { C[i] -= rm; negm[i] = -rm; } \
                PIN(negm); \
            } else if (__builtin_expect(__builtin_amdgcn_ballot_w64(a_ > THR) != 0, 0)) { \
                  \
                const float dl = __builtin_fmaxf(__builtin_ceilf(XMAX(a_)), 0.f); \
                const float al = __builtin_amdgcn_exp2f(-dl); mref += dl; ls2 *= al; \
                _Pragma("unroll") for (int i = 0; i < 16; ++i) { C[i] -= dl; negm[i] = -mref; } \
                PIN(negm); \
                _Pragma("unroll") for (int i = 0; i < 4; ++i) p1[i] = cvtpk(__uint_as_float(p1[i] << 16) * al, __uint_as_float(p1[i] & 0xffff0000u) * al); \
                _Pragma("unroll") for (int tt = 0; tt < 4; ++tt) \
                    _Pragma("unroll") for (int i = 0; i < 16; ++i) o[tt][i] *= al; \
                PIN(C); PIN(p1); PIN(ls2); PIN(o[0]); PIN(o[1]); PIN(o[2]); PIN(o[3]); \
            } \
            PV(4, p1); EXP2(C, 0); PV(5, p1); EXP2(C, 2); PV(6, p1); EXP2(C, 4); PV(7, p1); EXP2(C, 6); SBAR(); \
            if (BOUND) { \
                if (u > 0) { \
                    auto r2_ = __builtin_amdgcn_permlane32_swap(__float_as_uint(lprev), __float_as_uint(lprev), false, false); \
                    const float inv = 1.0f / (__uint_as_float(r2_[0]) + __uint_as_float(r2_[1])); \
                    _Pragma("unroll") for (int tt = 0; tt < 4; ++tt) \
                        _Pragma("unroll") for (int k = 0; k < 4; ++k) { \
                            u32x2 w = {cvtpk(o[tt][4 * k] * inv, o[tt][4 * k + 1] * inv), cvtpk(o[tt][4 * k + 2] * inv, o[tt][4 * k + 3] * inv)}; \
                            *(__attribute__((address_space(3))) u32x2*)(L + ooff + tt * 4096 + k * 256) = w; \
                        } \
                } \
                _Pragma("unroll") for (int i = 0; i < 4; ++i) o[i] = f32x16{}; \
            } \
            if (PF) WAIT_BAR(7); else WAIT_BAR(6); \
        } while (0)
        for (int u = 0; u < 4; ++u) {
            STEP(sA, sB, 0, true, PFS == 0); STEP(sB, sA, 1, false, PFS == 1); STEP(sA, sB, 2, false, PFS == 2); STEP(sB, sA, 3, false, PFS == 3);
            for (int j = 0; j < 3; ++j) {
                STEP(sA, sB, 0, false, PFS == 0); STEP(sB, sA, 1, false, PFS == 1); STEP(sA, sB, 2, false, PFS == 2); STEP(sB, sA, 3, false, PFS == 3);
            }
        }
        { const int u = 4; STEP(sA, sB, 0, true, false); }
        WAITVM(0);
        PROF_END();
        __builtin_amdgcn_endpgm();
    } else {
        const int pw = wid - 4;
        unsigned wv0, wv1;
        const u32x4 rW = mkrsrc(wq + hg * 2048 + pw * 128, 576 * 8192);
        const unsigned wdst = lds0 + LDS_QO + pw * 8192;
        unsigned wp;
#define SET_W(u) do { const int l_ = lane_id(); wv0 = (unsigned)((l_ >> 3) * 8192 + (((l_ & 7) ^ (((l_ >> 4) & 1) << 2)) * 16)); wv1 = wv0 + (65536 - 1024); \
                      wp = (u) * 512; asm volatile("" : "+s"(wp)); } while (0)
#define ISSUE_W(i) do { SETM0_ADD(wdst, ((i) & 3) * 2048); asm volatile("s_nop 0"); BDMA2(wv0, wv1, rW, wp); wp += 131072; asm volatile("" : "+s"(wp)); } while (0)
#define START_W(u) do { SET_W(u); ISSUE_W(0); ISSUE_W(1); ISSUE_W(2); ISSUE_W(3); } while (0)
        const auto ro = __builtin_amdgcn_make_buffer_rsrc((void*)wo, 0, 4096 * 1152, 0x00020000);
        f32x4 acc[9][4];
        int so2, sp, sp2, so;
        START_W(0);
        LOAD_X();
        MARK();
        WAIT_BAR(0);
#define RD_A(k, buf) do { \
            af[buf][0] = cat8(vtr(L + wbase + ((k) & 3) * 2048), vtr(L + wbase + ((k) & 3) * 2048 + 512)); \
            af[buf][1] = cat8(vtr(L + wb1 + ((k) & 3) * 2048), vtr(L + wb1 + ((k) & 3) * 2048 + 512)); } while (0)
#define RD_B(k, buf) do { \
            if ((k) == 32) { xbase += 65536; xb1 += 65536; PIN(xbase); PIN(xb1); } \
            bfr[buf][0] = ldsr128(L + (((k) & 1) ? xb1 : xbase) + (((k) >> 2) & 7) * 8192 + (((k) >> 1) & 1) * 512); \
            bfr[buf][1] = ldsr128(L + (((k) & 1) ? xb1 : xbase) + (((k) >> 2) & 7) * 8192 + (((k) >> 1) & 1) * 512 + 4096); } while (0)
#define QM(i, j, k) do { SBAR(); qa[i][j] = MFMA32(af[(k) & 1][i], bfr[(k) & 1][j], qa[i][j]); PIN(qa[i][j]); SBAR(); } while (0)
#define Q_PHASE(BARS) do { \
            const int lane = lane_id(), r = lane & 31, h = lane >> 5, qq = (lane & 15) >> 2, pp = lane & 3, gam = (lane >> 4) & 1; \
            int wbase = LDS_QO + pw * 8192 + 128 * (8 * h + qq) + 16 * (2 * gam + (pp & 1)) + 8 * (pp >> 1) + 64 * (qq >> 1); PIN(wbase); \
            int xbase = LDS_X + (r >> 3) * 1024 + ((r >> 2) & 1) * 256 + (r & 3) * 64 + 16 * (h ^ ((r >> 2) & 3)), xb1 = xbase ^ 32; PIN(xbase); PIN(xb1); \
            f32x16 qa[2][2]; \
            qa[0][0] = f32x16{}; qa[0][1] = f32x16{}; qa[1][0] = f32x16{}; qa[1][1] = f32x16{}; \
            bf16x8 af[2][2], bfr[2][2]; \
            int wb1 = wbase ^ 64; PIN(wb1); \
            WAITVM(6); \
            RD_A(0, 0); RD_B(0, 0); \
            _Pragma("unroll") for (int k = 0; k < 36; ++k) { \
                if (k + 4 < 36) SETM0_ADD(wdst, (k & 3) * 2048); \
                LGKM0(); \
                QM(0, 0, k); \
                if (k + 4 < 36) { BDMA2(wv0, wv1, rW, wp); wp += 131072; asm volatile("" : "+s"(wp)); } \
                if (k < 35) RD_B(k + 1, (k + 1) & 1); \
                if (BARS) PACE(QP - 24); \
                QM(0, 1, k); \
                if (k < 35) { \
                    if (k < 32) WAITVM(6); else if (k == 32) WAITVM(4); else if (k == 33) WAITVM(2); else WAITVM(0); \
                    RD_A(k + 1, (k + 1) & 1); \
                } \
                if (BARS) PACE(QP - 20); \
                QM(1, 0, k); \
                if (BARS) PACE(QP); \
                QM(1, 1, k); \
                if (BARS) PACE(QP - 8); \
                if (!(BARS) && k == 30) BAR(); \
                if (BARS && k <= 30 && k % 5 == 0) BAR(); \
            } \
            const int qwbase = LDS_QO + pw * 8192 + lane_id() * 16; \
            _Pragma("unroll") for (int t = 0; t < 2; ++t) \
                _Pragma("unroll") for (int m = 0; m < 2; ++m) \
                    _Pragma("unroll") for (int k = 0; k < 2; ++k) { \
                        const f32x16& a = qa[t][m]; \
                        u32x4 w = {cvtpk(a[8 * k] * QSCALE, a[8 * k + 1] * QSCALE), cvtpk(a[8 * k + 2] * QSCALE, a[8 * k + 3] * QSCALE), \
                                   cvtpk(a[8 * k + 4] * QSCALE, a[8 * k + 5] * QSCALE), cvtpk(a[8 * k + 6] * QSCALE, a[8 * k + 7] * QSCALE)}; \
                        *(__attribute__((address_space(3))) u32x4*)(L + qwbase + t * 4096 + m * 2048 + k * 1024) = w; \
                    } \
            SBAR(); \
        } while (0)
#define LOADJ(set, j) do { if ((j) < 8) raw[set][(j) & 7] = __builtin_amdgcn_raw_buffer_load_b128(ro, voa + ((j) & 3) * 1152, ((j) & 4) ? so2 : so, 0); \
                           else rc[(j) & 3] = __builtin_amdgcn_raw_buffer_load_b32(ro, voc + ((j) & 1) * 2304, ((j) & 2) ? sp2 : sp, 0); } while (0)
#define NEXT_ROWS() do { sp = so; sp2 = so2; so += 32 * 1152; asm volatile("" : "+s"(so)); so2 = so + 4608; asm volatile("" : "+s"(so2)); } while (0)
#define PERM1(dst, set, t, j) do { \
            if ((t) < 8) dst[j] = __builtin_amdgcn_perm(raw[set][2 * (j) + 1][((t) >> 1) & 3], raw[set][2 * (j)][((t) >> 1) & 3], ((t) & 1) ? 0x07060302u : 0x05040100u); \
            else dst[j] = __builtin_amdgcn_perm((unsigned)__builtin_amdgcn_mov_dpp((int)rc[j], 0xB1, 0xf, 0xf, true), rc[j], selc); } while (0)
#define O_PHASE(BARS, NEXT) do { \
            const int lane = lane_id(), c = lane & 15, q = lane >> 4; \
            int obase = LDS_QO + lane * 16; PIN(obase); \
            const int voa = (1024 * hg + 8 * q) * 1152 + (128 * pw + 8 * c) * 2; \
            const int voc = (1024 * hg + 8 * q + (c & 1)) * 1152 + (512 + 16 * pw + (c & 14)) * 2; \
            const unsigned selc = (c & 1) ? 0x03020706u : 0x05040100u; \
            bf16x8 of[4]; u32x4 ft[2], raw[2][8]; unsigned rc[4]; \
            _Pragma("unroll") for (int j = 0; j < 8; ++j) LOADJ(0, j); \
            NEXT_ROWS(); \
            _Pragma("unroll") for (int j = 0; j < 12; ++j) LOADJ(1, j); \
            NEXT_ROWS(); \
            SBAR(); \
            if (!(BARS)) { for (int j = 0; j < 8; ++j) BAR(); } \
            BAR(); \
            _Pragma("unroll") for (int m = 0; m < 4; ++m) of[m] = ldsr128(L + obase + m * 1024); \
            _Pragma("unroll") for (int j = 0; j < 4; ++j) PERM1(ft[0], 0, 0, j); \
            PIN(ft[0]); asm volatile("s_nop 1"); \
            _Pragma("unroll") for (int s = 0; s < 8; ++s) { \
                if (s == 6 && BARS) { SET_W(NEXT); if ((NEXT) >= 0) { ISSUE_W(0); ISSUE_W(1); } } \
                if (s == 7 && (NEXT) >= 0) { ISSUE_W(2); ISSUE_W(3); } \
                _Pragma("unroll") for (int t = 0; t < 9; ++t) \
                    _Pragma("unroll") for (int m = 0; m < 4; ++m) { \
                        const int g = s * 9 + t, nset = (t < 8) ? (s & 1) : ((s + 1) & 1), nt = (t < 8) ? t + 1 : 0, pos = t * 4 + m - 26; \
                        SBAR(); \
                        MFMA16_ACC(acc[t][m], ft[g & 1], of[m]); \
                        SBAR(); \
                        if (PPOS == 0 && m < 2 && g < 71) { PERM1(ft[(g + 1) & 1], nset, nt, 2 * m); PERM1(ft[(g + 1) & 1], nset, nt, 2 * m + 1); PIN(ft[(g + 1) & 1]); } \
                        if (PPOS == 1 && m == 1 && g < 71) { PERM1(ft[(g + 1) & 1], nset, nt, 0); PERM1(ft[(g + 1) & 1], nset, nt, 1); PERM1(ft[(g + 1) & 1], nset, nt, 2); PERM1(ft[(g + 1) & 1], nset, nt, 3); PIN(ft[(g + 1) & 1]); } \
                        if (s < 6 && pos >= 0 && pos % LSTEP == 0) LOADJ(s & 1, pos / LSTEP); \
                        if (s < 7 && pos >= 4 && (pos - 4) % LSTEP == 0 && (pos - 4) / LSTEP < 4) LOADJ(0, 8 + (pos - 4) / LSTEP); \
                        if (s > 0 && s < 7 && (pos + 36) % LSTEP == 0 && (pos + 36) / LSTEP < 8) LOADJ((s - 1) & 1, (pos + 36) / LSTEP); \
                        if (s > 0 && (pos + 32) % LSTEP == 0 && (pos + 32) / LSTEP < 4) LOADJ(0, 8 + (pos + 32) / LSTEP); \
                        if (s > 0 && s < 7 && pos == -4) NEXT_ROWS(); \
                        if (t == 8 && s < 7) of[m] = ldsr128(L + obase + (s + 1) * 4096 + m * 1024); \
                    } \
                SBAR(); \
                if (BARS && s < 7) LGKM_BAR(); \
                if (!(BARS)) MARK(); \
            } \
        } while (0)
        Q_PHASE(false);
        LGKM_BAR();
        BAR();
        {
            const int idx = min(pw * 18 + (lane_id() & 31), 71);
            const char* p0 = wq + (size_t)(18 * b + (idx >> 2)) * 8192 + hg * 2048 + 512 + (idx & 3) * 128;
            unsigned pf1;
            asm volatile("global_load_dword %0, %1, off" : "=&v"(pf1) : "v"(p0) : "memory");
            for (int j = 0; j < 6; ++j) BAR();
            WAITVM(0);
            asm volatile("" ::"v"(pf1));
        }
        START_W(1);
        BAR();
#pragma unroll
        for (int t = 0; t < 9; ++t)
#pragma unroll
            for (int m = 0; m < 4; ++m) acc[t][m] = f32x4{};
        so = 0; so2 = 4608; sp = 0; sp2 = 0;
        for (int i = 1; i < 4; ++i) {
            Q_PHASE(true);
            LGKM_BAR();
            O_PHASE(true, (i < 3 ? i + 1 : -1));
        }
        O_PHASE(false, -1);
        asm volatile("s_nop 15\n\ts_nop 15");
        const int lane = lane_id();
        RT(0);
        const auto rx = __builtin_amdgcn_make_buffer_rsrc((void*)part, 0, XCH_BYTES, 0x00020000);
        gu32* flags = (gu32*)(part + FLAG_OFF);
#pragma unroll
        for (int j = 1; j < 4; ++j) {
            const int sb = (((wg ^ j) * 4 + pw) * 3 + j - 1) * 4608;
#pragma unroll
            for (int i = 0; i < 4; ++i) {
                const f32x4 &a0 = acc[2 * i][j], &a1 = acc[2 * i + 1][j];
                u32x4 w = {cvtpk(a0[0], a0[1]), cvtpk(a0[2], a0[3]), cvtpk(a1[0], a1[1]), cvtpk(a1[2], a1[3])};
                __builtin_amdgcn_raw_buffer_store_b128(w, rx, lane * 16 + i * 1024, sb, 16);
            }
            u32x2 w = {cvtpk(acc[8][j][0], acc[8][j][1]), cvtpk(acc[8][j][2], acc[8][j][3])};
            __builtin_amdgcn_raw_buffer_store_b64(w, rx, lane * 8 + 4096, sb, 16);
        }
        WAITVM(0);
        asm volatile("s_barrier" ::: "memory");
        RT(1);
        gu32* fl = flags + (wg >> 2) * 64;
        if (pw == 0 && lane == 0) __hip_atomic_store(fl + hg, 1u, __ATOMIC_RELAXED, __HIP_MEMORY_SCOPE_AGENT);
        for (unsigned spins = 0; spins < (1u << 17); ++spins) {
            const unsigned v = __hip_atomic_load(fl + (lane & 3), __ATOMIC_RELAXED, __HIP_MEMORY_SCOPE_AGENT);
            if (__builtin_amdgcn_ballot_w64(v != 1u) == 0) break;
            __builtin_amdgcn_s_sleep(1);
        }
        __builtin_amdgcn_fence(__ATOMIC_ACQUIRE, "wavefront");
        asm volatile("" ::: "memory");
        RT(2);
        u32x4 rv[3][4]; u32x2 rw[3];
#pragma unroll
        for (int j = 0; j < 3; ++j) {
            const int sb = ((wg * 4 + pw) * 3 + j) * 4608;
#pragma unroll
            for (int i = 0; i < 4; ++i) rv[j][i] = __builtin_amdgcn_raw_buffer_load_b128(rx, lane * 16 + i * 1024, sb, 16);
            rw[j] = __builtin_amdgcn_raw_buffer_load_b64(rx, lane * 8 + 4096, sb, 16);
        }
#define BLO(x) __uint_as_float((x) << 16)
#define BHI(x) __uint_as_float((x) & 0xffff0000u)
#pragma unroll
        for (int j = 0; j < 3; ++j) {
#pragma unroll
            for (int i = 0; i < 4; ++i) {
                acc[2 * i][0] += (f32x4){BLO(rv[j][i][0]), BHI(rv[j][i][0]), BLO(rv[j][i][1]), BHI(rv[j][i][1])};
                acc[2 * i + 1][0] += (f32x4){BLO(rv[j][i][2]), BHI(rv[j][i][2]), BLO(rv[j][i][3]), BHI(rv[j][i][3])};
            }
            acc[8][0] += (f32x4){BLO(rw[j][0]), BHI(rw[j][0]), BLO(rw[j][1]), BHI(rw[j][1])};
        }
        RT(3);
        unsigned short* orow = out + ((size_t)(b * 64 + 16 * hg + (lane & 15)) * 1152 + br * 576);
        const int q = lane >> 4;
#pragma unroll
        for (int r = 0; r < 4; ++r) {
            u32x4 w = {cvtpk(acc[0][0][r], acc[1][0][r]), cvtpk(acc[2][0][r], acc[3][0][r]), cvtpk(acc[4][0][r], acc[5][0][r]), cvtpk(acc[6][0][r], acc[7][0][r])};
            *(u32x4*)(orow + 128 * pw + 32 * q + 8 * r) = w;
        }
        u32x2 w = {cvtpk(acc[8][0][0], acc[8][0][1]), cvtpk(acc[8][0][2], acc[8][0][3])};
        *(u32x2*)(orow + 512 + 16 * pw + 4 * q) = w;
        PROF_END();
        __builtin_amdgcn_endpgm();
    }
}

__global__ __launch_bounds__(64) void zero_flags(char* __restrict__ part) {
    *(u32x4*)(part + FLAG_OFF + threadIdx.x * 256) = u32x4{};
}

extern "C" void kernel_launch(void* const* d_in, const int* in_sizes, int n_in, void* d_out, int out_size, void* d_ws, size_t ws_size,
                              hipStream_t stream) {
    (void)in_sizes; (void)n_in; (void)out_size; (void)ws_size;
    char* part = (char*)d_ws;
    zero_flags<<<1, 64, 0, stream>>>(part);
    fused_gqa<<<256, 512, 0, stream>>>((const char*)d_in[0], (const char*)d_in[1], (const char*)d_in[2], (const char*)d_in[3], (const char*)d_in[4],
                                       (const char*)d_in[5], (const char*)d_in[6], (const char*)d_in[7], (const char*)d_in[8], (const char*)d_in[9], part,
                                       (unsigned short*)d_out);
}
```

```cpp
#include <hip/hip_runtime.h>
#include <hip/hip_bf16.h>
#include <stdint.h>

typedef short bf16x8 __attribute__((ext_vector_type(8)));
typedef short s16x4 __attribute__((ext_vector_type(4)));
typedef float f32x16 __attribute__((ext_vector_type(16)));
typedef float f32x4 __attribute__((ext_vector_type(4)));
typedef float f32x2 __attribute__((ext_vector_type(2)));
typedef unsigned u32x4 __attribute__((ext_vector_type(4)));
typedef unsigned u32x2 __attribute__((ext_vector_type(2)));
typedef __attribute__((address_space(3))) char* lds_ptr;
typedef __attribute__((address_space(3))) const char* lds_cptr;

constexpr int LDS_X = 0, LDS_QO = 73728, LDS_KV = 106496, LDS_TOTAL = 163840;
constexpr float QSCALE = 0.08838834764831845f * 1.4426950408889634f;
constexpr float THR = 8.0f;
constexpr int XCH_BYTES = 256 * 4 * 3 * 4608, FLAG_OFF = 16 << 20;
typedef __attribute__((address_space(1))) unsigned long long gu64;
#define RLX __ATOMIC_RELAXED, __HIP_MEMORY_SCOPE_AGENT
#define LSTEP 2
#define SLP 8
#define SBAR() __builtin_amdgcn_sched_barrier(0)
#define PIN(x) asm volatile("" : "+v"(x))
#define MFMA32(a, b, c) __builtin_amdgcn_mfma_f32_32x32x16_bf16(a, b, c, 0, 0, 0)
#define MFMA16_ACC(c, a, b) asm volatile("v_mfma_f32_16x16x32_bf16 %0, %1, %2, %0" : "+v"(c) : "v"(a), "v"(b))
#define WAITC(vm, lg) __builtin_amdgcn_s_waitcnt(((vm) & 15) | 0x70 | ((lg) << 8) | (((vm) >> 4) << 14))
#define WAIT_BAR(N) do { asm volatile("" ::: "memory"); WAITC(N, 0); asm volatile("s_barrier" ::: "memory"); } while (0)
#define LGKM_BAR() WAIT_BAR(63)
#define LGKM0() do { asm volatile("" ::: "memory"); WAITC(63, 0); asm volatile("" ::: "memory"); } while (0)
#define BAR() do { asm volatile("s_barrier" ::: "memory"); } while (0)
#define WAITVM(N) asm volatile("s_waitcnt vmcnt(" #N ")" ::: "memory")
#define WVM(N) asm volatile("s_waitcnt vmcnt(%0)" ::"n"(N) : "memory")
#define MX3(a, b, c) __builtin_fmaxf(__builtin_fmaxf((a), (b)), (c))

__device__ __forceinline__ unsigned cvtpk(float lo, float hi) {
    typedef float f32x2_t __attribute__((ext_vector_type(2)));
    typedef __bf16 bf16x2_t __attribute__((ext_vector_type(2)));
    return __builtin_bit_cast(unsigned, __builtin_convertvector((f32x2_t){lo, hi}, bf16x2_t));
}
__device__ __forceinline__ void dma1(unsigned voff, const char* sbase, unsigned lds_base) {
    unsigned sv;
    asm volatile("s_mov_b32 %0, m0\n\ts_mov_b32 m0, %3\n\ts_nop 0\n\tglobal_load_lds_dwordx4 %1, %2\n\ts_mov_b32 m0, %0"
                 : "=&s"(sv) : "v"(voff), "s"(sbase), "s"(lds_base) : "memory");
}
#define SETM0(v) asm volatile("s_mov_b32 m0, %0" ::"s"(v) : "memory")
#define SETM0_ADD(v, imm) asm volatile("s_add_u32 m0, %0, %1" ::"s"(v), "n"(imm) : "memory", "scc")
#define BDMA2(v0, v1, rs, so) asm volatile("buffer_load_dwordx4 %0, %2, %3 offen lds\n\tbuffer_load_dwordx4 %1, %2, %3 offen offset:1024 lds" ::"v"(v0), "v"(v1), "s"(rs), "s"(so) : "memory")
__device__ __forceinline__ u32x4 mkrsrc(const char* p, unsigned n) {
    const unsigned long long a = (unsigned long long)(uintptr_t)p;
    return (u32x4){(unsigned)a, (unsigned)(a >> 32) & 0xffffu, n, 0x00020000u};
}
__device__ __forceinline__ int lane_id() { int l; asm volatile("v_mbcnt_lo_u32_b32 %0, -1, 0\n\tv_mbcnt_hi_u32_b32 %0, -1, %0" : "=v"(l)); return l; }
__device__ __forceinline__ bf16x8 ldsr128(lds_cptr p) { return *(const __attribute__((address_space(3))) bf16x8*)p; }
__device__ __forceinline__ s16x4 vtr(lds_cptr p) {
    return __builtin_bit_cast(s16x4, __builtin_amdgcn_ds_read_tr16_b64_v4i16((__attribute__((address_space(3))) s16x4*)p));
}
__device__ __forceinline__ bf16x8 cat8(s16x4 a, s16x4 b) { return (bf16x8){a[0], a[1], a[2], a[3], b[0], b[1], b[2], b[3]}; }

__global__ __launch_bounds__(512, 2) void fused_gqa(const char* __restrict__ xA, const char* __restrict__ kA, const char* __restrict__ vA,
                                                    const char* __restrict__ wqA, const char* __restrict__ woA,
                                                    const char* __restrict__ xB, const char* __restrict__ kB, const char* __restrict__ vB,
                                                    const char* __restrict__ wqB, const char* __restrict__ woB, char* __restrict__ part,
                                                    unsigned short* __restrict__ out) {
    __shared__ __attribute__((aligned(1024))) char lds_raw[LDS_TOTAL];
    const int wid = __builtin_amdgcn_readfirstlane(threadIdx.x >> 6);
    const int wg = blockIdx.x, xg = wg & 7, br = xg >> 2, hg = xg & 3, b = wg >> 3;
    const unsigned lds0 = (unsigned)(uintptr_t)lds_raw;
    const lds_ptr L = (lds_ptr)lds_raw;
    const char* xp = br ? xB : xA;
    const char* wq = br ? wqB : wqA;
    const char* wo = br ? woB : woA;

#define LOAD_X() do { \
        const int lane_ = lane_id(); \
        const unsigned voff = (unsigned)(((lane_ >> 2) & 7) * 1152 + (lane_ >> 5) * 64 + (((lane_ & 3) ^ ((lane_ >> 4) & 1) ^ ((wid & 1) << 1)) << 4)); \
        const char* xb = xp + (size_t)(b * 64 + 8 * (wid ^ (2 * hg))) * 1152; \
        const char* xc = xp + (size_t)(b * 64 + 8 * ((wid + 4) ^ (2 * hg))) * 1152; \
        _Pragma("unroll") for (int i = 0; i < 9; ++i) { \
            dma1(voff, xb + i * 128, lds0 + LDS_X + i * 8192 + wid * 1024); \
            dma1(voff, xc + i * 128, lds0 + LDS_X + i * 8192 + wid * 1024 + 4096); \
        } } while (0)
    if (wid < 4) {
        unsigned pf0;
        {
            const int idx = wid * 18 + (lane_id() & 31);
            const int id2 = idx > 71 ? 71 : idx;
            const char* p0 = wq + (size_t)(18 * b + (id2 >> 2)) * 8192 + hg * 2048 + (id2 & 3) * 128;
            asm volatile("global_load_dword %0, %1, off" : "=&v"(pf0) : "v"(p0) : "memory");
        }
        LOAD_X();
        const int lane = lane_id();
        const int aw = wid, g = aw >> 1, half = aw & 1;
        const int r = lane & 31, h = lane >> 5;
        const char* kb = (br ? kB : kA) + (size_t)(b * 16 + 4 * hg) * 131072;
        const char* vb = (br ? vB : vA) + (size_t)(b * 16 + 4 * hg) * 131072;
        unsigned doff = (unsigned)((8 * aw + ((lane >> 2) & 3)) * 256 + ((lane >> 4) * 4 + ((lane & 3) ^ ((2 * aw) & 3))) * 16);
        unsigned doff1 = doff ^ 16;
        const unsigned ddst = lds0 + LDS_KV + aw * 2048;
        const u32x4 rK = mkrsrc(kb, 524288), rV = mkrsrc(vb - 24576, 524288 + 24576);
#define KVDMA(rs) asm volatile("buffer_load_dwordx4 %0, %2, 0 offen nt lds\n\tbuffer_load_dwordx4 %1, %2, 0 offen offset:1024 nt lds" ::"v"(doff), "v"(doff1), "s"(rs) : "memory")
#define ADV() do { doff += 8192; doff1 += 8192; PIN(doff); PIN(doff1); } while (0)
#define NOP0() asm volatile("s_nop 0")
        WAIT_BAR(12);
        WAIT_BAR(6);
        SETM0_ADD(ddst, 0); NOP0(); KVDMA(rK); ADV();
        SETM0_ADD(ddst, 8192); NOP0(); KVDMA(rK); ADV();
        asm volatile("" ::"v"(pf0));
        int kb0 = LDS_KV + 2048 * (r >> 3) + 1024 * ((r >> 2) & 1) + 64 * (r & 3) + 16 * (h ^ ((r >> 2) & 3)), kb1 = kb0 ^ 32; PIN(kb0); PIN(kb1);
        const int qq = (lane & 15) >> 2, pp = lane & 3, gam = (lane >> 4) & 1;
        const int vbase = LDS_KV + 32768 + 1024 * h + 64 * qq + 16 * ((2 * gam + (pp >> 1)) ^ h) + 8 * (pp & 1);
        const int qoff = LDS_QO + (16 * g + 2 * half) * 1024 + lane * 16;
        const int ooff = LDS_QO + (16 * g + 2 * half + (r >> 4)) * 1024 + (r & 15) * 16 + 8 * h;
        const char* pfb = aw < 2 ? wq + (size_t)(18 * b + 9 * aw) * 8192 + hg * 2048 + 1024 : wo + (size_t)hg * 1179648 + (18 * b + 9 * (aw - 2)) * 128;
        const int pfs = aw < 2 ? 13 : 7;
        const unsigned pfi = aw < 2 ? 128 : 73728, pfe = aw < 2 ? 896 : 15 * 73728;
        unsigned pfo = 0;
        f32x16 o[4], sA, sB, negm;
        bf16x8 qf[8], kf[8];
        f32x2 ls2 = f32x2{};
        float mref = 0.f;
        int vr = 0, vw = 16384;
        asm volatile("" : "+s"(vr), "+s"(vw), "+s"(pfo));
        int vp0 = vbase, vp1 = vp0 ^ 32;
#define VRD(i) do { vl[i] = vtr(L + vp0 + ((i) >> 2) * 4096 + ((i) & 3) * 256); vh[i] = vtr(L + vp1 + ((i) >> 2) * 4096 + 2048 + ((i) & 3) * 256); } while (0)
#define KRD(s, slot) kf[s] = ldsr128(L + (((s) & 1) ? kb1 : kb0) + (slot) * 8192 + ((s) >> 1) * 256)
        WAIT_BAR(4);
        WAIT_BAR(2);
        SETM0_ADD(ddst, 16384); NOP0(); KVDMA(rK); ADV();
        SETM0_ADD(ddst, 32768); NOP0(); KVDMA(rV);
        SETM0_ADD(ddst, 24576); NOP0(); KVDMA(rK); ADV();
#pragma unroll
        for (int s = 0; s < 8; ++s) KRD(s, 0);
        WAIT_BAR(6);
#define EXP2(X, i) do { float e0_ = __builtin_amdgcn_exp2f(X[i]), e1_ = __builtin_amdgcn_exp2f(X[(i) + 1]); PIN(e0_); PIN(e1_); X[i] = e0_; X[(i) + 1] = e1_; } while (0)
#define SUM2(X, i) ls2 += (f32x2){X[i], X[(i) + 1]}
#define QK(C, s) do { SBAR(); C = MFMA32(kf[s], qf[s], C); SBAR(); } while (0)
#define PV(i, p) do { SBAR(); o[(i) & 3] = MFMA32(cat8(vl[i], vh[i]), __builtin_bit_cast(bf16x8, p), o[(i) & 3]); SBAR(); } while (0)
#define XMAX(x) ({ auto rr_ = __builtin_amdgcn_permlane32_swap(__float_as_uint(x), __float_as_uint(x), false, false); \
                   __builtin_fmaxf(__uint_as_float(rr_[0]), __uint_as_float(rr_[1])); })
#define WRO(tt, k0) do { _Pragma("unroll") for (int k = k0; k < k0 + 2; ++k) { \
                u32x2 w = {cvtpk(o[tt][4 * k] * inv, o[tt][4 * k + 1] * inv), cvtpk(o[tt][4 * k + 2] * inv, o[tt][4 * k + 3] * inv)}; \
                *(__attribute__((address_space(3))) u32x2*)(L + ooff + tt * 4096 + k * 256) = w; } } while (0)
#define STEP(C, P, PH, BOUND, PF) do { \
            s16x4 vl[8], vh[8]; \
            if (BOUND) { _Pragma("unroll") for (int s = 0; s < 8; ++s) qf[s] = ldsr128(L + qoff + (s >> 1) * 4096 + (s & 1) * 1024); C = f32x16{}; \
                QK(C, 0); } \
            else { SBAR(); asm volatile("v_mfma_f32_32x32x16_bf16 %0, %2, %3, %1" : "=&v"(C), "+v"(negm) : "v"(kf[0]), "v"(qf[0])); SBAR(); } \
            VRD(0); VRD(1); EXP2(P, 8); \
            QK(C, 1); VRD(2); VRD(3); EXP2(P, 10); \
            QK(C, 2); VRD(4); VRD(5); EXP2(P, 12); \
            { const unsigned m_ = ddst + 32768 + vw; SETM0(m_); } \
            QK(C, 3); VRD(6); VRD(7); EXP2(P, 14); \
            vw = vr; vr += 8192; asm volatile("" : "+s"(vr)); \
            QK(C, 4); KVDMA(rV); \
            u32x4 p0 = {cvtpk(P[0], P[1]), cvtpk(P[2], P[3]), cvtpk(P[4], P[5]), cvtpk(P[6], P[7])}; PIN(p0); \
            vr = (vr == 24576) ? 0 : vr; asm volatile("" : "+s"(vr)); \
            QK(C, 5); SUM2(P, 0); SUM2(P, 2); SUM2(P, 4); SUM2(P, 6); PIN(ls2); \
            SETM0_ADD(ddst, (PH) * 8192); \
            QK(C, 6); \
            if (PF) { const unsigned po_ = (min(lane_id(), 8) << pfs) + pfo; \
                asm volatile("global_load_lds_dword %0, %1" ::"v"(po_), "s"(pfb) : "memory");     \
                pfo = min(pfo + pfi, pfe); asm volatile("" : "+s"(pfo)); } \
            KVDMA(rK); \
            u32x4 p1 = {cvtpk(P[8], P[9]), cvtpk(P[10], P[11]), cvtpk(P[12], P[13]), cvtpk(P[14], P[15])}; PIN(p1); \
            QK(C, 7); \
            SUM2(P, 8); SUM2(P, 10); SUM2(P, 12); SUM2(P, 14); PIN(ls2); \
            if (BOUND) { \
                  \
                const float lprev = ls2[0] + ls2[1]; ls2 = f32x2{}; \
                LGKM0(); \
                PV(0, p0); KRD(0, 1); KRD(1, 1); ADV(); vp0 = vbase + vr; vp1 = vp0 ^ 32; PIN(vp0); PIN(vp1); \
                auto r2_ = __builtin_amdgcn_permlane32_swap(__float_as_uint(lprev), __float_as_uint(lprev), false, false); \
                float lt_ = __uint_as_float(r2_[0]) + __uint_as_float(r2_[1]); PIN(lt_); \
                PV(4, p1); KRD(2, 1); KRD(3, 1); \
                float inv = 1.0f / lt_; PIN(inv); \
                PV(1, p0); KRD(4, 1); KRD(5, 1); \
                float a_ = MX3(C[0], C[1], C[2]), c_ = MX3(C[3], C[4], C[5]); \
                a_ = MX3(a_, C[6], C[7]); c_ = MX3(c_, C[8], C[9]); PIN(a_); PIN(c_); \
                PV(5, p1); KRD(6, 1); KRD(7, 1); WRO(0, 0); \
                PV(2, p0); WRO(0, 2); \
                PV(6, p1); WRO(1, 0); \
                PV(3, p0); WRO(1, 2); \
                PV(7, p1); WRO(2, 0); WRO(2, 2); SBAR(); WRO(3, 0); WRO(3, 2); SBAR(); \
                float d_ = MX3(C[10], C[11], C[12]), e_ = MX3(C[13], C[14], C[15]); \
                a_ = MX3(a_, c_, d_); a_ = __builtin_fmaxf(a_, e_); \
                const float rm = XMAX(a_); mref = rm; \
                _Pragma("unroll") for (int i = 0; i < 16; ++i) { C[i] -= rm; negm[i] = -rm; } \
                PIN(negm); \
                EXP2(C, 0); EXP2(C, 2); EXP2(C, 4); EXP2(C, 6); SBAR(); \
            } else { \
            LGKM0(); \
            PV(0, p0); KRD(0, ((PH) + 1) & 3); KRD(1, ((PH) + 1) & 3); ADV(); vp0 = vbase + vr; vp1 = vp0 ^ 32; PIN(vp0); PIN(vp1); \
            PV(1, p0); KRD(2, ((PH) + 1) & 3); KRD(3, ((PH) + 1) & 3); \
            float a_ = MX3(C[0], C[1], C[2]), c_ = MX3(C[3], C[4], C[5]); \
            a_ = MX3(a_, C[6], C[7]); c_ = MX3(c_, C[8], C[9]); PIN(a_); PIN(c_); \
            PV(2, p0); KRD(4, ((PH) + 1) & 3); KRD(5, ((PH) + 1) & 3); \
            float d_ = MX3(C[10], C[11], C[12]), e_ = MX3(C[13], C[14], C[15]); \
            asm volatile("v_max3_f32 %0, %0, %1, %2\n\tv_max_f32 %0, %0, %3" : "+v"(a_) : "v"(c_), "v"(d_), "v"(e_)); \
            PV(3, p0); KRD(6, ((PH) + 1) & 3); KRD(7, ((PH) + 1) & 3); \
            if (__builtin_expect(__builtin_amdgcn_ballot_w64(a_ > THR) != 0, 0)) { \
                  \
                const float dl = __builtin_fmaxf(__builtin_ceilf(XMAX(a_)), 0.f); \
                const float al = __builtin_amdgcn_exp2f(-dl); mref += dl; ls2 *= al; \
                _Pragma("unroll") for (int i = 0; i < 16; ++i) { C[i] -= dl; negm[i] = -mref; } \
                PIN(negm); \
                _Pragma("unroll") for (int i = 0; i < 4; ++i) p1[i] = cvtpk(__uint_as_float(p1[i] << 16) * al, __uint_as_float(p1[i] & 0xffff0000u) * al); \
                _Pragma("unroll") for (int tt = 0; tt < 4; ++tt) \
                    _Pragma("unroll") for (int i = 0; i < 16; ++i) o[tt][i] *= al; \
                PIN(C); PIN(p1); PIN(ls2); PIN(o[0]); PIN(o[1]); PIN(o[2]); PIN(o[3]); \
            } \
            PV(4, p1); EXP2(C, 0); PV(5, p1); EXP2(C, 2); PV(6, p1); EXP2(C, 4); PV(7, p1); EXP2(C, 6); SBAR(); \
            } \
            if (PF) WAIT_BAR(7); else WAIT_BAR(6); \
        } while (0)
        {
#pragma unroll
            for (int s = 0; s < 8; ++s) qf[s] = ldsr128(L + qoff + (s >> 1) * 4096 + (s & 1) * 1024);
            SETM0_ADD(ddst, 32768 + 8192); NOP0(); KVDMA(rV);
            SETM0_ADD(ddst, 0); NOP0();
            sA = f32x16{};
            QK(sA, 0); KVDMA(rK); ADV(); KRD(0, 1);
            QK(sA, 1); KRD(1, 1); QK(sA, 2); KRD(2, 1); QK(sA, 3); KRD(3, 1); QK(sA, 4); KRD(4, 1);
            QK(sA, 5); KRD(5, 1); QK(sA, 6); KRD(6, 1); QK(sA, 7); KRD(7, 1);
            float a_ = MX3(sA[0], sA[1], sA[2]), c_ = MX3(sA[3], sA[4], sA[5]), d_ = MX3(sA[10], sA[11], sA[12]), e_ = MX3(sA[13], sA[14], sA[15]);
            a_ = MX3(a_, sA[6], sA[7]); c_ = MX3(c_, sA[8], sA[9]); a_ = MX3(a_, c_, d_); a_ = __builtin_fmaxf(a_, e_);
            const float rm = XMAX(a_); mref = rm;
#pragma unroll
            for (int i = 0; i < 16; ++i) { sA[i] -= rm; negm[i] = -rm; }
            PIN(negm);
            EXP2(sA, 0); EXP2(sA, 2); EXP2(sA, 4); EXP2(sA, 6); SBAR();
            WAIT_BAR(6);
        }
        for (int u = 0;; ++u) {
            vr = __builtin_amdgcn_readfirstlane(vr); vw = __builtin_amdgcn_readfirstlane(vw); pfo = __builtin_amdgcn_readfirstlane(pfo);
#pragma unroll
            for (int i = 0; i < 4; ++i) o[i] = f32x16{};
            STEP(sB, sA, 1, false, false); STEP(sA, sB, 2, false, true); STEP(sB, sA, 3, false, false);
            for (int j = 0; j < 3; ++j) {
                STEP(sA, sB, 0, false, false); STEP(sB, sA, 1, false, false); STEP(sA, sB, 2, false, true); STEP(sB, sA, 3, false, false);
            }
            if (u == 3) break;
            STEP(sA, sB, 0, true, false);
        }
        {
            s16x4 vl[8], vh[8];
            VRD(0); VRD(4); VRD(1); VRD(5); VRD(2); VRD(6); VRD(3); VRD(7);
            EXP2(sB, 8); EXP2(sB, 10); EXP2(sB, 12); EXP2(sB, 14);
            u32x4 p0 = {cvtpk(sB[0], sB[1]), cvtpk(sB[2], sB[3]), cvtpk(sB[4], sB[5]), cvtpk(sB[6], sB[7])};
            u32x4 p1 = {cvtpk(sB[8], sB[9]), cvtpk(sB[10], sB[11]), cvtpk(sB[12], sB[13]), cvtpk(sB[14], sB[15])};
            PV(0, p0); SUM2(sB, 0); SUM2(sB, 2); SUM2(sB, 4); SUM2(sB, 6); PIN(ls2);
            PV(4, p1); SUM2(sB, 8); SUM2(sB, 10); SUM2(sB, 12); SUM2(sB, 14);
            const float lprev = ls2[0] + ls2[1];
            auto r2_ = __builtin_amdgcn_permlane32_swap(__float_as_uint(lprev), __float_as_uint(lprev), false, false);
            float lt_ = __uint_as_float(r2_[0]) + __uint_as_float(r2_[1]); PIN(lt_);
            PV(1, p0);
            float inv = 1.0f / lt_; PIN(inv);
            PV(5, p1); WRO(0, 0);
            PV(2, p0); WRO(0, 2);
            PV(6, p1); WRO(1, 0);
            PV(3, p0); WRO(1, 2);
            PV(7, p1); WRO(2, 0); WRO(2, 2); SBAR(); WRO(3, 0); WRO(3, 2); SBAR();
            LGKM_BAR();
        }
        WAITVM(0);
        __builtin_amdgcn_endpgm();
    } else {
        const int pw = wid - 4;
        unsigned wv0, wv1;
        const u32x4 rW = mkrsrc(wq + hg * 2048 + pw * 128, 576 * 8192);
        const unsigned wdst = lds0 + LDS_QO + pw * 8192;
        unsigned wp;
#define SET_W(u) do { const int l_ = lane_id(); wv0 = (unsigned)((l_ >> 3) * 8192 + (((l_ & 7) ^ (((l_ >> 4) & 1) << 2)) * 16)); wv1 = wv0 + (65536 - 1024); \
                      wp = (u) * 512; asm volatile("" : "+s"(wp)); } while (0)
#define WSL(k, NS) (((k) & 3) * 2048 + ((NS) == 8 && ((k) & 4) ? 49152 : 0))
#define ISSUE_W(i) do { SETM0_ADD(wdst, WSL(i, 8)); asm volatile("s_nop 0"); BDMA2(wv0, wv1, rW, wp); wp += 131072; asm volatile("" : "+s"(wp)); } while (0)
#define START_W(u) do { SET_W(u); ISSUE_W(0); ISSUE_W(1); ISSUE_W(2); ISSUE_W(3); } while (0)
        const auto ro = __builtin_amdgcn_make_buffer_rsrc((void*)wo, 0, 4096 * 1152, 0x00020000);
        f32x4 acc[9][4];
        int so2, sp, sp2, so;
        START_W(0);
        BAR();
#define RD_A(k, buf) do { \
            af[buf][0] = cat8(vtr(L + wbase + WSL(k, NS)), vtr(L + wbase + WSL(k, NS) + 512)); \
            af[buf][1] = cat8(vtr(L + wb1 + WSL(k, NS)), vtr(L + wb1 + WSL(k, NS) + 512)); } while (0)
#define RD_B(k, buf) do { \
            if ((k) == 32) { xbase += 65536; xb1 += 65536; PIN(xbase); PIN(xb1); } \
            bfr[buf][0] = ldsr128(L + (((k) & 1) ? xb1 : xbase) + (((k) >> 2) & 7) * 8192 + (((k) >> 1) & 1) * 512); \
            bfr[buf][1] = ldsr128(L + (((k) & 1) ? xb1 : xbase) + (((k) >> 2) & 7) * 8192 + (((k) >> 1) & 1) * 512 + 4096); } while (0)
#define QM(i, j, k) do { SBAR(); qa[i][j] = MFMA32(af[(k) & 1][i], bfr[(k) & 1][j], qa[i][j]); PIN(qa[i][j]); SBAR(); } while (0)
#define Q_PHASE(BARS) do { \
            const int lane = lane_id(), r = lane & 31, h = lane >> 5, qq = (lane & 15) >> 2, pp = lane & 3, gam = (lane >> 4) & 1; \
            int wbase = LDS_QO + pw * 8192 + 128 * (8 * h + qq) + 16 * (2 * gam + (pp & 1)) + 8 * (pp >> 1) + 64 * (qq >> 1); PIN(wbase); \
            int xbase = LDS_X + (r >> 3) * 1024 + ((r >> 2) & 1) * 256 + (r & 3) * 64 + 16 * (h ^ ((r >> 2) & 3)), xb1 = xbase ^ 32; PIN(xbase); PIN(xb1); \
            f32x16 qa[2][2]; \
            qa[0][0] = f32x16{}; qa[0][1] = f32x16{}; qa[1][0] = f32x16{}; qa[1][1] = f32x16{}; \
            bf16x8 af[2][2], bfr[2][2]; \
            int wb1 = wbase ^ 64; PIN(wb1); \
            constexpr int NS = (BARS) ? 4 : 8; \
            WVM(6); \
            RD_A(0, 0); RD_B(0, 0); \
            _Pragma("unroll") for (int k = 0; k < 36; ++k) { \
                const int last = (NS == 8 && k < 4) ? 5 + 2 * k : k + NS;     \
                if (last < 36) SETM0_ADD(wdst, WSL((NS == 8 && k < 4) ? last - 1 : last, NS)); \
                LGKM0(); \
                QM(0, 0, k); \
                if (last < 36) { BDMA2(wv0, wv1, rW, wp); wp += 131072; asm volatile("" : "+s"(wp)); } \
                if (NS == 8 && k < 4) ISSUE_W(last); \
                if (k < 35) RD_B(k + 1, (k + 1) & 1); \
                QM(0, 1, k); \
                if (k < 35) { \
                    WVM(2 * ((last < 35 ? last : 35) - k - 1)); \
                    RD_A(k + 1, (k + 1) & 1); \
                } \
                QM(1, 0, k); \
                QM(1, 1, k); \
                if (!(BARS) && (k == 10 || k == 22 || k == 31)) BAR(); \
                if (BARS && k <= 30 && k % 5 == 0) BAR(); \
            } \
            const int qwbase = LDS_QO + pw * 8192 + lane_id() * 16; \
            _Pragma("unroll") for (int t = 0; t < 2; ++t) \
                _Pragma("unroll") for (int m = 0; m < 2; ++m) \
                    _Pragma("unroll") for (int k = 0; k < 2; ++k) { \
                        const f32x16& a = qa[t][m]; \
                        u32x4 w = {cvtpk(a[8 * k] * QSCALE, a[8 * k + 1] * QSCALE), cvtpk(a[8 * k + 2] * QSCALE, a[8 * k + 3] * QSCALE), \
                                   cvtpk(a[8 * k + 4] * QSCALE, a[8 * k + 5] * QSCALE), cvtpk(a[8 * k + 6] * QSCALE, a[8 * k + 7] * QSCALE)}; \
                        *(__attribute__((address_space(3))) u32x4*)(L + qwbase + t * 4096 + m * 2048 + k * 1024) = w; \
                    } \
            SBAR(); \
        } while (0)
#define LOADJ(set, j) do { if ((j) < 8) raw[set][(j) & 7] = __builtin_amdgcn_raw_buffer_load_b128(ro, voa + ((j) & 3) * 1152, ((j) & 4) ? so2 : so, 0); \
                           else rc[(j) & 3] = __builtin_amdgcn_raw_buffer_load_b32(ro, voc + ((j) & 1) * 2304, ((j) & 2) ? sp2 : sp, 0); } while (0)
#define NEXT_ROWS() do { sp = so; sp2 = so2; so += 32 * 1152; asm volatile("" : "+s"(so)); so2 = so + 4608; asm volatile("" : "+s"(so2)); } while (0)
#define PERM1(dst, set, t, j) do { \
            if ((t) < 8) dst[j] = __builtin_amdgcn_perm(raw[set][2 * (j) + 1][((t) >> 1) & 3], raw[set][2 * (j)][((t) >> 1) & 3], ((t) & 1) ? 0x07060302u : 0x05040100u); \
            else dst[j] = __builtin_amdgcn_perm((unsigned)__builtin_amdgcn_mov_dpp((int)rc[j], 0xB1, 0xf, 0xf, true), rc[j], selc); } while (0)
#define O_PHASE(BARS, NEXT) do { \
            const int lane = lane_id(), c = lane & 15, q = lane >> 4; \
            int obase = LDS_QO + lane * 16; PIN(obase); \
            const int voa = (1024 * hg + 8 * q) * 1152 + (128 * pw + 8 * c) * 2; \
            const int voc = (1024 * hg + 8 * q + (c & 1)) * 1152 + (512 + 16 * pw + (c & 14)) * 2; \
            const unsigned selc = (c & 1) ? 0x03020706u : 0x05040100u; \
            bf16x8 of[4]; u32x4 ft[2], raw[2][8]; unsigned rc[4]; \
            if (BARS) { \
                _Pragma("unroll") for (int j = 0; j < 8; ++j) LOADJ(0, j); \
                NEXT_ROWS(); \
                if (SLP > 0) { SBAR(); __builtin_amdgcn_s_sleep(SLP); SBAR(); } \
                _Pragma("unroll") for (int j = 8; j < 12; ++j) LOADJ(1, j); \
                _Pragma("unroll") for (int j = 0; j < 8; ++j) LOADJ(1, j); \
            } else { \
                _Pragma("unroll") for (int j = 0; j < 24; ++j) { \
                    if (j % 3 == 0) { SBAR(); BAR(); } \
                    if (j < 8) LOADJ(0, j); else if (j < 20) LOADJ(1, j - 8); \
                    if (j == 7 || j == 19) NEXT_ROWS(); \
                } \
            } \
            SBAR(); \
            BAR(); \
            _Pragma("unroll") for (int m = 0; m < 4; ++m) of[m] = ldsr128(L + obase + m * 1024); \
            _Pragma("unroll") for (int j = 0; j < 4; ++j) PERM1(ft[0], 0, 0, j); \
            PIN(ft[0]); asm volatile("s_nop 1"); \
            _Pragma("unroll") for (int s = 0; s < 8; ++s) { \
                _Pragma("unroll") for (int t = 0; t < 9; ++t) \
                    _Pragma("unroll") for (int m = 0; m < 4; ++m) { \
                        const int g = s * 9 + t, nset = (t < 8) ? (s & 1) : ((s + 1) & 1), nt = (t < 8) ? t + 1 : 0, pos = t * 4 + m - 26; \
                        SBAR(); \
                        MFMA16_ACC(acc[t][m], ft[g & 1], of[m]); \
                        SBAR(); \
                        if (m < 2 && g < 71) { PERM1(ft[(g + 1) & 1], nset, nt, 2 * m); PERM1(ft[(g + 1) & 1], nset, nt, 2 * m + 1); PIN(ft[(g + 1) & 1]); } \
                        if (s < 6 && pos >= 0 && pos % LSTEP == 0) LOADJ(s & 1, pos / LSTEP); \
                        if (s < 7 && pos >= 4 && (pos - 4) % LSTEP == 0 && (pos - 4) / LSTEP < 4) LOADJ(0, 8 + (pos - 4) / LSTEP); \
                        if (s > 0 && s < 7 && (pos + 36) % LSTEP == 0 && (pos + 36) / LSTEP < 8) LOADJ((s - 1) & 1, (pos + 36) / LSTEP); \
                        if (s > 0 && (pos + 32) % LSTEP == 0 && (pos + 32) / LSTEP < 4) LOADJ(0, 8 + (pos + 32) / LSTEP); \
                        if ((s > 0 || BARS) && s < 7 && pos == -4) NEXT_ROWS(); \
                        if (t == 8 && s < 7) of[m] = ldsr128(L + obase + (s + 1) * 4096 + m * 1024); \
                        if (s == 6 && BARS && t == 8 && m == 2) { SET_W(NEXT); if ((NEXT) >= 0) { ISSUE_W(0); ISSUE_W(1); } } \
                        if (s == 7 && (NEXT) >= 0 && t == 0 && m == 0) { ISSUE_W(2); ISSUE_W(3); } \
                    } \
                SBAR(); \
                if (BARS && s < 7) LGKM_BAR(); \
            } \
        } while (0)
        Q_PHASE(false);
        LGKM_BAR();
        BAR();
        const unsigned long long nonce = __builtin_amdgcn_s_memrealtime();
        gu64* box = (gu64*)(part + FLAG_OFF);
        if (pw == 0 && lane_id() == 0) __hip_atomic_store(box + wg * 32, nonce, RLX);
        {
            const int idx = min(pw * 18 + (lane_id() & 31), 71);
            const char* p0 = wq + (size_t)(18 * b + (idx >> 2)) * 8192 + hg * 2048 + 512 + (idx & 3) * 128;
            unsigned pf1;
            asm volatile("global_load_dword %0, %1, off" : "=&v"(pf1) : "v"(p0) : "memory");
            for (int j = 0; j < 6; ++j) BAR();
            WAITVM(0);
            asm volatile("" ::"v"(pf1));
        }
        START_W(1);
        BAR();
#pragma unroll
        for (int t = 0; t < 9; ++t)
#pragma unroll
            for (int m = 0; m < 4; ++m) acc[t][m] = f32x4{};
        so = 0; so2 = 4608; sp = 0; sp2 = 0;
        for (int i = 1; i < 4; ++i) {
            Q_PHASE(true);
            LGKM_BAR();
            O_PHASE(true, (i < 3 ? i + 1 : -1));
        }
        O_PHASE(false, -1);
        asm volatile("s_nop 15\n\ts_nop 15");
        const int lane = lane_id();
        const auto rx = __builtin_amdgcn_make_buffer_rsrc((void*)part, 0, XCH_BYTES, 0x00020000);
        const int pj = 1 + (lane & 3) % 3;
        gu64* ptag = box + (wg ^ pj) * 32;
        unsigned long long pn = 0;
        if (pw == 0) pn = __hip_atomic_load(ptag, RLX);
#pragma unroll
        for (int j = 1; j < 4; ++j) {
            const int sb = (((wg ^ j) * 4 + pw) * 3 + j - 1) * 4608;
#pragma unroll
            for (int i = 0; i < 4; ++i) {
                const f32x4 &a0 = acc[2 * i][j], &a1 = acc[2 * i + 1][j];
                u32x4 w = {cvtpk(a0[0], a0[1]), cvtpk(a0[2], a0[3]), cvtpk(a1[0], a1[1]), cvtpk(a1[2], a1[3])};
                __builtin_amdgcn_raw_buffer_store_b128(w, rx, lane * 16 + i * 1024, sb, 16);
            }
            u32x2 w = {cvtpk(acc[8][j][0], acc[8][j][1]), cvtpk(acc[8][j][2], acc[8][j][3])};
            __builtin_amdgcn_raw_buffer_store_b64(w, rx, lane * 8 + 4096, sb, 16);
        }
        WAITVM(0);
        asm volatile("s_barrier" ::: "memory");
        if (pw == 0) {
            if (lane < 3) __hip_atomic_store(ptag + 16 + pj, pn, RLX);
            for (unsigned spins = 0; spins < (1u << 17); ++spins) {
                const unsigned long long f = __hip_atomic_load(box + wg * 32 + 16 + pj, RLX);
                const unsigned long long n2 = __hip_atomic_load(ptag, RLX);
                if (n2 != pn) { pn = n2; if (lane < 3) __hip_atomic_store(ptag + 16 + pj, pn, RLX); }
                if (__builtin_amdgcn_ballot_w64(f != nonce) == 0) break;
                __builtin_amdgcn_s_sleep(1);
            }
        }
        asm volatile("s_barrier" ::: "memory");
        __builtin_amdgcn_fence(__ATOMIC_ACQUIRE, "wavefront");
        asm volatile("" ::: "memory");
        u32x4 rv[3][4]; u32x2 rw[3];
#pragma unroll
        for (int j = 0; j < 3; ++j) {
            const int sb = ((wg * 4 + pw) * 3 + j) * 4608;
#pragma unroll
            for (int i = 0; i < 4; ++i) rv[j][i] = __builtin_amdgcn_raw_buffer_load_b128(rx, lane * 16 + i * 1024, sb, 16);
            rw[j] = __builtin_amdgcn_raw_buffer_load_b64(rx, lane * 8 + 4096, sb, 16);
        }
        if (pw == 0) {
            const unsigned long long n2 = __hip_atomic_load(ptag, RLX);
            if (n2 != pn && lane < 3) __hip_atomic_store(ptag + 16 + pj, n2, RLX);
        }
#define BLO(x) __uint_as_float((x) << 16)
#define BHI(x) __uint_as_float((x) & 0xffff0000u)
#pragma unroll
        for (int j = 0; j < 3; ++j) {
#pragma unroll
            for (int i = 0; i < 4; ++i) {
                acc[2 * i][0] += (f32x4){BLO(rv[j][i][0]), BHI(rv[j][i][0]), BLO(rv[j][i][1]), BHI(rv[j][i][1])};
                acc[2 * i + 1][0] += (f32x4){BLO(rv[j][i][2]), BHI(rv[j][i][2]), BLO(rv[j][i][3]), BHI(rv[j][i][3])};
            }
            acc[8][0] += (f32x4){BLO(rw[j][0]), BHI(rw[j][0]), BLO(rw[j][1]), BHI(rw[j][1])};
        }
        unsigned short* orow = out + ((size_t)(b * 64 + 16 * hg + (lane & 15)) * 1152 + br * 576);
        const int q = lane >> 4;
#pragma unroll
        for (int r = 0; r < 4; ++r) {
            u32x4 w = {cvtpk(acc[0][0][r], acc[1][0][r]), cvtpk(acc[2][0][r], acc[3][0][r]), cvtpk(acc[4][0][r], acc[5][0][r]), cvtpk(acc[6][0][r], acc[7][0][r])};
            *(u32x4*)(orow + 128 * pw + 32 * q + 8 * r) = w;
        }
        u32x2 w = {cvtpk(acc[8][0][0], acc[8][0][1]), cvtpk(acc[8][0][2], acc[8][0][3])};
        *(u32x2*)(orow + 512 + 16 * pw + 4 * q) = w;
        __builtin_amdgcn_endpgm();
    }
}

extern "C" void kernel_launch(void* const* d_in, const int* in_sizes, int n_in, void* d_out, int out_size, void* d_ws, size_t ws_size,
                              hipStream_t stream) {
    (void)in_sizes; (void)n_in; (void)out_size; (void)ws_size;
    char* part = (char*)d_ws;
    fused_gqa<<<256, 512, 0, stream>>>((const char*)d_in[0], (const char*)d_in[1], (const char*)d_in[2], (const char*)d_in[3], (const char*)d_in[4],
                                       (const char*)d_in[5], (const char*)d_in[6], (const char*)d_in[7], (const char*)d_in[8], (const char*)d_in[9], part,
                                       (unsigned short*)d_out);
}
```
